```python
import torch

def _softmax(x, axis=-1):
    return torch.softmax(x, dim=axis)
def _gelu(x):
    return torch.nn.functional.gelu(x, approximate="tanh")
def _silu(x):
    return torch.nn.functional.silu(x)
def _sigmoid(x):
    return torch.sigmoid(x)
def _layernorm(x, axis=-1, eps=1e-5):
    m = x.mean(dim=axis, keepdim=True)
    v = x.var(dim=axis, keepdim=True, unbiased=False)
    return (x - m) / torch.sqrt(v + eps)
def _scatter_add(tgt, idx, src, axis=0):
    return tgt.clone().index_add_(axis, idx, src)
def _segment_sum(src, seg, n):
    out = torch.zeros((n,) + src.shape[1:], dtype=src.dtype)
    return out.index_add_(0, seg, src)
def _pad(x, pw):
    flat = tuple(v for pair in reversed(pw) for v in pair)
    return torch.nn.functional.pad(x, flat)

kernel_name = 'concat_heads__mha_block_cached__mha_block_cached__near_ridge'

def setup_inputs():

    _w = (lambda a: (lambda t: t.to(torch.bfloat16) if t.is_floating_point() else t)(torch.as_tensor(a)))
    def _A_setup():
        B, Sq, D = 8, 112, 1024
        H, Sk, Dh = 32, 512, 128
        return {
            "x": _w((lambda *s: torch.randn(*s, dtype=torch.bfloat16) * 0.8)(B, Sq, D)),
            "k_cache": _w((lambda *s: torch.randn(*s, dtype=torch.bfloat16) * 0.8)(B, H, Sk, Dh)),
            "v_cache": _w((lambda *s: torch.randn(*s, dtype=torch.bfloat16) * 0.05)(B, H, Sk, Dh)),
            "wq": _w((lambda *s: torch.randn(*s, dtype=torch.bfloat16) * 0.8)(D, H * Dh) * (D ** -0.5)),
            "wo": _w((lambda *s: torch.randn(*s, dtype=torch.bfloat16) * 0.05)(H * Dh, D) * ((H * Dh) ** -0.5)),
        }
    _A_in = _A_setup()

    def _B_setup():
        B, Sq, D = 8, 112, 1024
        H, Sk, Dh = 32, 512, 128
        return {
            "x": _w((lambda *s: torch.randn(*s, dtype=torch.bfloat16) * 0.8)(B, Sq, D)),
            "k_cache": _w((lambda *s: torch.randn(*s, dtype=torch.bfloat16) * 0.8)(B, H, Sk, Dh)),
            "v_cache": _w((lambda *s: torch.randn(*s, dtype=torch.bfloat16) * 0.05)(B, H, Sk, Dh)),
            "wq": _w((lambda *s: torch.randn(*s, dtype=torch.bfloat16) * 0.8)(D, H * Dh) * (D ** -0.5)),
            "wo": _w((lambda *s: torch.randn(*s, dtype=torch.bfloat16) * 0.05)(H * Dh, D) * ((H * Dh) ** -0.5)),
        }
    _B_in = _B_setup()

    return {
        "A_x": _A_in["x"],
        "A_k_cache": _A_in["k_cache"],
        "A_v_cache": _A_in["v_cache"],
        "A_wq": _A_in["wq"],
        "A_wo": _A_in["wo"],
        "B_x": _B_in["x"],
        "B_k_cache": _B_in["k_cache"],
        "B_v_cache": _B_in["v_cache"],
        "B_wq": _B_in["wq"],
        "B_wo": _B_in["wo"],
    }

def reference(A_x, A_k_cache, A_v_cache, A_wq, A_wo, B_x, B_k_cache, B_v_cache, B_wq, B_wo):
    def _A_ref(x, k_cache, v_cache, wq, wo):
        B, Sq, D = x.shape
        H, Dh = 32, 128
        q = (lambda x, *d: x.permute(*d))((x @ wq).reshape(B, Sq, H, Dh), 0, 2, 1, 3)
        scores = torch.einsum("bhqd,bhkd->bhqk", q, k_cache) * (Dh ** -0.5)
        p = _softmax(scores, axis=-1)
        o = torch.einsum("bhqk,bhkd->bhqd", p, v_cache)
        return (lambda x, *d: x.permute(*d))(o, 0, 2, 1, 3).reshape(B, Sq, H * Dh) @ wo

    def _B_ref(x, k_cache, v_cache, wq, wo):
        B, Sq, D = x.shape
        H, Dh = 32, 128
        q = (lambda x, *d: x.permute(*d))((x @ wq).reshape(B, Sq, H, Dh), 0, 2, 1, 3)
        scores = torch.einsum("bhqd,bhkd->bhqk", q, k_cache) * (Dh ** -0.5)
        p = _softmax(scores, axis=-1)
        o = torch.einsum("bhqk,bhkd->bhqd", p, v_cache)
        return (lambda x, *d: x.permute(*d))(o, 0, 2, 1, 3).reshape(B, Sq, H * Dh) @ wo

    a = _A_ref(A_x, A_k_cache, A_v_cache, A_wq, A_wo)
    b = _B_ref(B_x, B_k_cache, B_v_cache, B_wq, B_wo)
    return torch.concatenate([a, b], axis=-1)
```

```cpp
#include <hip/hip_runtime.h>
#include <hip/hip_bf16.h>
typedef __hip_bfloat16 bf16;
static __device__ __forceinline__ float b2f(bf16 x){ return __bfloat162float(x); }

__global__ void gemm_naive(const bf16* A, const bf16* B, bf16* C, int M, int N, int K, int ldc){
  __shared__ float As[16][17]; __shared__ float Bs[16][17];
  int tx=threadIdx.x, ty=threadIdx.y; int row=blockIdx.y*16+ty, col=blockIdx.x*16+tx; float acc=0;
  for(int k0=0;k0<K;k0+=16){ As[ty][tx]=b2f(A[(size_t)row*K+k0+tx]); Bs[ty][tx]=b2f(B[(size_t)(k0+ty)*N+col]); __syncthreads();
    for(int k=0;k<16;++k) acc+=As[ty][k]*Bs[k][tx]; __syncthreads(); }
  C[(size_t)row*ldc+col]=__float2bfloat16(acc);
}
__global__ void attn_naive(const bf16* Q, const bf16* Kc, const bf16* Vc, bf16* O){
  int q=blockIdx.x%112, h=(blockIdx.x/112)%32, b=blockIdx.x/(112*32); int t=threadIdx.x;
  __shared__ float qs[128]; __shared__ float s[512]; __shared__ float red[128];
  qs[t]=b2f(Q[((size_t)b*112+q)*4096+h*128+t]); __syncthreads();
  const bf16* Kp=Kc+((size_t)b*32+h)*512*128; const bf16* Vp=Vc+((size_t)b*32+h)*512*128;
  float mx=-1e30f;
  for(int j=t;j<512;j+=128){ float a=0; for(int d=0;d<128;++d) a+=qs[d]*b2f(Kp[j*128+d]); a*=0.08838834764831845f; s[j]=a; mx=fmaxf(mx,a);}
  red[t]=mx; __syncthreads(); for(int o=64;o>0;o>>=1){ if(t<o) red[t]=fmaxf(red[t],red[t+o]); __syncthreads(); } mx=red[0]; __syncthreads();
  float sum=0; for(int j=t;j<512;j+=128){ float e=__expf(s[j]-mx); s[j]=e; sum+=e;} red[t]=sum; __syncthreads();
  for(int o=64;o>0;o>>=1){ if(t<o) red[t]+=red[t+o]; __syncthreads(); } sum=red[0];
  float acc=0; for(int j=0;j<512;++j) acc+=s[j]*b2f(Vp[j*128+t]);
  O[((size_t)b*112+q)*4096+h*128+t]=__float2bfloat16(acc/sum);
}
extern "C" void kernel_launch(void* const* d_in, const int* in_sizes, int n_in, void* d_out, int out_size, void* d_ws, size_t ws_size, hipStream_t stream){
  bf16* out=(bf16*)d_out; bf16* Q=(bf16*)d_ws; bf16* O=Q+(size_t)896*4096;
  for(int br=0;br<2;++br){
    const bf16* x=(const bf16*)d_in[br*5+0]; const bf16* k=(const bf16*)d_in[br*5+1]; const bf16* v=(const bf16*)d_in[br*5+2];
    const bf16* wq=(const bf16*)d_in[br*5+3]; const bf16* wo=(const bf16*)d_in[br*5+4];
    gemm_naive<<<dim3(4096/16,896/16),dim3(16,16),0,stream>>>(x,wq,Q,896,4096,1024,4096);
    attn_naive<<<8*32*112,128,0,stream>>>(Q,k,v,O);
    gemm_naive<<<dim3(1024/16,896/16),dim3(16,16),0,stream>>>(O,wo,out+br*1024,896,1024,4096,2048);
  }
}
```

```cpp
#include <hip/hip_runtime.h>
#include <hip/hip_bf16.h>
#include <stdint.h>

typedef __attribute__((ext_vector_type(8))) short bf16x8;
typedef __attribute__((ext_vector_type(4))) short s16x4;
typedef __attribute__((ext_vector_type(4))) float f32x4;
typedef __attribute__((ext_vector_type(2))) float f32x2;
typedef __attribute__((ext_vector_type(4))) unsigned u32x4;
typedef __attribute__((ext_vector_type(2))) __bf16 bf16x2_t;
typedef short v4i16_t __attribute__((ext_vector_type(4)));
typedef unsigned long long u64;
#define LAS __attribute__((address_space(3)))
#define GAS __attribute__((address_space(1)))
#define DI __device__ __forceinline__
#define MFMA16(a, b, c) __builtin_amdgcn_mfma_f32_16x16x32_bf16((a), (b), (c), 0, 0, 0)
#define SBAR() __builtin_amdgcn_sched_barrier(0)
#define RLX_AGENT __ATOMIC_RELAXED, __HIP_MEMORY_SCOPE_AGENT

#define W_NT 1
#define W_NT2 0
#define ROT 1
#define THR 8.0f

constexpr int LDS_BYTES = 163840;
constexpr int SLOT = 16384;
constexpr int WOFF = 65536, WST = 16384;
constexpr int XOFF = 131072, XST = 7168;
constexpr int QXOFF = 65536;
constexpr int Q1OFF = 131072;
constexpr int STOFF = 159744;
constexpr size_t WS_O = 0;
constexpr size_t WS_PART = 16u << 20;
constexpr size_t PART_TILE = 16 * 28 * 1024;
constexpr size_t WS_FLAG = 64u << 20;

template <int NT>
DI void glds(const void* sbase, unsigned voff, unsigned ldsdst) {
  if (NT == 1) asm volatile("s_mov_b32 m0, %2\n\ts_nop 3\n\tglobal_load_lds_dwordx4 %0, %1 nt" ::"v"(voff), "s"(sbase), "s"(ldsdst) : "memory");
  else if (NT == 2) asm volatile("s_mov_b32 m0, %2\n\ts_nop 3\n\tglobal_load_lds_dwordx4 %0, %1 sc1" ::"v"(voff), "s"(sbase), "s"(ldsdst) : "memory");
  else asm volatile("s_mov_b32 m0, %2\n\ts_nop 3\n\tglobal_load_lds_dwordx4 %0, %1" ::"v"(voff), "s"(sbase), "s"(ldsdst) : "memory");
}
template <int N> DI void vbar() { asm volatile("s_waitcnt vmcnt(%0)\n\ts_barrier" ::"i"(N) : "memory"); }
template <int N> DI void wait_vm() { asm volatile("s_waitcnt vmcnt(%0)" ::"i"(N) : "memory"); }
DI void lbar() { asm volatile("s_waitcnt lgkmcnt(0)\n\ts_barrier" ::: "memory"); }
DI void bar() { asm volatile("s_barrier" ::: "memory"); }
DI unsigned cvtpk(float lo, float hi) { const f32x2 v = {lo, hi}; return __builtin_bit_cast(unsigned, __builtin_convertvector(v, bf16x2_t)); }
#define STAMP(i) do {} while (0)
#define CSTAMP(i) do {} while (0)
#define HSTAMP(i) do {} while (0)
DI s16x4 trread(LAS const char* p) { return __builtin_bit_cast(s16x4, __builtin_amdgcn_ds_read_tr16_b64_v4i16((LAS v4i16_t*)p)); }
DI bf16x8 ldsr(LAS const char* p) { return *(LAS const bf16x8*)p; }
DI bf16x8 cat8(s16x4 a, s16x4 b) { return __builtin_shufflevector(a, b, 0, 1, 2, 3, 4, 5, 6, 7); }
DI int fsw(int row) { return (row & 3) | ((((row >> 2) ^ (row >> 3)) & 1) << 2); }
DI u64 mix64(u64 z) { z += 0x9E3779B97F4A7C15ull; z = (z ^ (z >> 30)) * 0xBF58476D1CE4E5B9ull; z = (z ^ (z >> 27)) * 0x94D049BB133111EBull; return z ^ (z >> 31); }

DI u32x4 pack32(f32x4 a, f32x4 b) {
  auto r0 = __builtin_amdgcn_permlane32_swap(cvtpk(a[0], a[1]), cvtpk(b[0], b[1]), false, false);
  auto r1 = __builtin_amdgcn_permlane32_swap(cvtpk(a[2], a[3]), cvtpk(b[2], b[3]), false, false);
  auto s0 = __builtin_amdgcn_permlane16_swap(r0[0], r0[1], false, false);
  auto s1 = __builtin_amdgcn_permlane16_swap(r1[0], r1[1], false, false);
  u32x4 o = {s0[0], s1[0], s0[1], s1[1]};
  return o;
}
DI float rowmax4(float mx) {
  auto r = __builtin_amdgcn_permlane32_swap(__float_as_uint(mx), __float_as_uint(mx), false, false);
  mx = __builtin_fmaxf(__uint_as_float(r[0]), __uint_as_float(r[1]));
  auto r2 = __builtin_amdgcn_permlane16_swap(__float_as_uint(mx), __float_as_uint(mx), false, false);
  return __builtin_fmaxf(__uint_as_float(r2[0]), __uint_as_float(r2[1]));
}
DI float max4(float m, const f32x4& c) { return __builtin_fmaxf(__builtin_fmaxf(__builtin_fmaxf(m, c[0]), c[1]), __builtin_fmaxf(c[2], c[3])); }

template <bool VM>
DI void c_stage(f32x4 (&acc)[7][4], bf16x8 (&X)[7], const bf16x8 (&W)[4], bf16x8 (&Wn)[4], LAS const char* xr, LAS const char* const (&wr)[8], int cs, int ns) {
  s16x4 lo[4];
#pragma unroll
  for (int gp = 0; gp < 28; ++gp) {
    const int mt = gp >> 2, e = gp & 3;
    acc[mt][e] = MFMA16(W[e], X[mt], acc[mt][e]);
    SBAR();
    if (gp == 0) X[6] = ldsr(xr + cs * XST + 6 * 1024);
    if (gp == 5) { if (VM) asm volatile("s_waitcnt vmcnt(0) lgkmcnt(0)\n\ts_barrier" ::: "memory"); else lbar(); }
    if (gp >= 6 && gp < 22 && (gp & 1) == 0) { const int k = (gp - 6) >> 1;
      if ((k & 1) == 0) lo[k >> 1] = trread(wr[k >> 1] + ns * WST); else Wn[k >> 1] = cat8(lo[k >> 1], trread(wr[4 + (k >> 1)] + ns * WST + 1024)); }
    if (gp >= 7 && (gp & 3) == 3) X[mt - 1] = ldsr(xr + ns * XST + (mt - 1) * 1024);
    SBAR();
  }
}
DI void c_gemm(f32x4 (&acc)[7][4], LAS char* L, int wid, int lane, bool xlin) {
  const int q = lane & 15, g = lane >> 4;
  LAS const char* xr = L + XOFF + (xlin ? lane * 16 : q * 64 + ((g ^ ((-(q >> 2)) & 3)) << 4));
  LAS const char* wr[8];
  { const int row = 8 * g + (q >> 2), p = q & 3, f = fsw(row), nt0 = 4 * (wid & 1);
    LAS const char* wb = L + WOFF + (wid >> 1) * 8192 + row * 256 + p * 8;
#pragma unroll
    for (int e = 0; e < 4; ++e) { wr[e] = wb + (((nt0 + e) ^ f) << 5); wr[4 + e] = wb + (((nt0 + e) ^ f ^ 4) << 5); } }
#pragma unroll
  for (int mt = 0; mt < 7; ++mt)
#pragma unroll
    for (int e = 0; e < 4; ++e) acc[mt][e] = (f32x4){0.f, 0.f, 0.f, 0.f};
  bf16x8 X[7], Wa[4], Wb[4];
  bar();
#pragma unroll
  for (int e = 0; e < 4; ++e) Wa[e] = cat8(trread(wr[e]), trread(wr[4 + e] + 1024));
#pragma unroll
  for (int mt = 0; mt < 6; ++mt) X[mt] = ldsr(xr + mt * 1024);
#pragma unroll 1
  for (int s = 0; s < 32; s += 4) {
    c_stage<false>(acc, X, Wa, Wb, xr, wr, 0, 1);
    c_stage<false>(acc, X, Wb, Wa, xr, wr, 1, 2);
    c_stage<false>(acc, X, Wa, Wb, xr, wr, 2, 3);
    c_stage<true>(acc, X, Wb, Wa, xr, wr, 3, 0);
  }
}

#define PIN(x) asm volatile("" : "+v"(x))
DI float mx3(float a, float b, float c) { return __builtin_elementwise_maximum(__builtin_elementwise_maximum(a, b), c); }
DI void vlbar() { asm volatile("s_waitcnt vmcnt(0) lgkmcnt(0)\n\ts_barrier" ::: "memory"); }

struct HA { const char* k0; const char* v0; unsigned kv[4], vv[4]; unsigned d; };
DI int kv_tile(int T, int& isv) { isv = (T & 1) == 0 && T > 0; const int n = isv ? (T - 2) >> 1 : (T + 1) >> 1; return (n >> 3) * (16 * 131072) + (n & 7) * SLOT; }
template <int NG>
DI void h_kv(const HA& A, int T) {
  int isv; const unsigned so = kv_tile(T, isv);
  const unsigned d = A.d + (T & 3) * SLOT;
  if (isv) {
#pragma unroll
    for (int i = 0; i < 4 * NG; ++i) glds<1>(A.v0 + so + (i >> 2) * 4096, A.vv[i & 3], d + i * 1024);
  } else {
#pragma unroll
    for (int i = 0; i < 4 * NG; ++i) glds<1>(A.k0 + so + (i >> 2) * 4096, A.kv[i & 3], d + i * 1024);
  }
}
DI void h_kv_setup(HA& A, const char* k0, const char* v0, int row0  , unsigned lds0, int lane) {
  A.k0 = k0 + row0 * 256; A.v0 = v0 + row0 * 256; A.d = lds0 + row0 * 256;
#pragma unroll
  for (int i = 0; i < 4; ++i) { const int row = 4 * i + (lane >> 4); A.kv[i] = row * 256 + (((lane & 15) ^ row) << 4); A.vv[i] = row * 256 + (((lane & 15) ^ (fsw(row) << 1)) << 4); }
}
DI void wait_flags4(GAS u64* f, u64 e, int lane) {
#pragma unroll 1
  for (unsigned spins = 0; spins < (1u << 17); ++spins) {
    const u64 v = __hip_atomic_load(f + (lane & 3), RLX_AGENT);
    if (__all(v == e)) break;
    __builtin_amdgcn_s_sleep(1);
  }
}
#define TS2(i) do {} while (0)
template <int NQ> struct AttnS { f32x4 O[NQ][8], Ls[NQ], nm[NQ]; bf16x8 Qf[NQ][4]; bf16x8 kf[8]; float mx[NQ]; };
struct AttnX { HA H; GAS u64* oflag; u64 ep; int c; bool w7; };

template <int NQ, bool VM> DI void a_bar(const AttnX& X, int T, int lane) {
  if (NQ == 2) { if (VM) vlbar(); else lbar(); }
  else {
    if (T < 29 || T == 30) h_kv<2>(X.H, T + 2);
    if (T < 29) wait_vm<16>(); else if (T == 29 || T == 32) wait_vm<0>();
    lbar();
    if (T == 20 && X.w7 && lane == 0) __hip_atomic_store(X.oflag + X.c, X.ep, RLX_AGENT);
  }
}
template <int NQ, int PAR>
DI void a_iter(AttnS<NQ>& A, f32x4 (&So)[NQ][2], f32x4 (&Sn)[NQ][2], LAS const char* const (&ka)[4], LAS const char* const (&va)[8], const AttnX& X, int n, int lane) {
  constexpr int VS = (PAR ? 0 : 2) * SLOT, KS2 = (PAR ? 1 : 3) * SLOT;
  const bf16x8 ones = {0x3F80, 0x3F80, 0x3F80, 0x3F80, 0x3F80, 0x3F80, 0x3F80, 0x3F80};
  u32x4 pw[NQ]; s16x4 vl[8], vh[8];
#pragma unroll
  for (int sl = 0; sl < 34; ++sl) {
    if (sl < 16) { const int f = sl >> 1, t = sl & 1, kt = f >> 2, s = f & 3;
      if (t < NQ) Sn[t][kt] = MFMA16(A.kf[f], A.Qf[t][s], s == 0 ? A.nm[t] : Sn[t][kt]); }
    else { const int t = (sl - 16) / 9, r = (sl - 16) % 9;
      if (t < NQ) { const bf16x8 pf = __builtin_bit_cast(bf16x8, pw[t]);
        if (r == 0) A.Ls[t] = MFMA16(ones, pf, A.Ls[t]); else A.O[t][r - 1] = MFMA16(cat8(vl[r - 1], vh[r - 1]), pf, A.O[t][r - 1]); } }
    { const int t = sl >> 4, e = sl & 15;
      if (t < NQ && e < 8) { float x = __builtin_amdgcn_exp2f(So[t][e >> 2][e & 3]); PIN(x); So[t][e >> 2][e & 3] = x; }
      if (t < NQ && e >= 2 && e <= 8 && (e & 1) == 0) { const int p = (e >> 1) - 1;
        unsigned w = cvtpk(So[t][p >> 1][2 * (p & 1)], So[t][p >> 1][2 * (p & 1) + 1]); PIN(w); pw[t][p] = w; } }
    if (sl >= 8 && sl < 16) { const int z = sl - 8; vl[z] = trread(va[z] + VS); vh[z] = trread(va[z] + VS + 4096); }
    if (sl >= 25 && sl < 33) { const int z = sl - 25, t = z & 1, m = z >> 1;
      A.kf[z] = ldsr(ka[z & 3] + KS2 + (z >> 2) * 4096);
      if (t < NQ) { float x = mx3(m == 0 ? -3.0e38f : A.mx[t], Sn[t][m >> 1][2 * (m & 1)], Sn[t][m >> 1][2 * (m & 1) + 1]); PIN(x); A.mx[t] = x; } }
    SBAR();
    if (sl == 7) { a_bar<NQ, false>(X, 2 * n + 2, lane); SBAR(); }
    if (sl == 24) { a_bar<NQ, true>(X, 2 * n + 3, lane); SBAR(); }
  }
}
template <int NQ>
DI void a_check(AttnS<NQ>& A, f32x4 (&Sn)[NQ][2], bool first) {
  if (first || __any(__builtin_fmaxf(A.mx[0], A.mx[NQ - 1]) > THR)) {
#pragma unroll
    for (int t = 0; t < NQ; ++t) {
      float d = rowmax4(A.mx[t]);
      if (!first) d = __builtin_fmaxf(d, 0.f);
      const float f = first ? 0.f : __builtin_amdgcn_exp2f(-d);
      A.nm[t] -= d;
      Sn[t][0] -= d; Sn[t][1] -= d;
#pragma unroll
      for (int dt = 0; dt < 8; ++dt) A.O[t][dt] *= f;
      A.Ls[t] *= f;
    }
  }
}
template <int NQ>
DI void a_loadq(AttnS<NQ>& A, LAS const char* Qi, int qg, int lane) {
#pragma unroll
  for (int t = 0; t < NQ; ++t)
#pragma unroll
    for (int s = 0; s < 4; ++s) A.Qf[t][s] = ldsr(Qi + ((2 * qg + t) * 4 + s) * 1024 + lane * 16);
}
template <int NQ, class F>
DI void c_attn(LAS char* L, int wid, int lane, char* orow  , const AttnX& X, F&& tail) {
  const int q = lane & 15, g = lane >> 4, qg = wid & 3, kh = wid >> 2;
  LAS const char* ka[4]; LAS const char* va[8];
#pragma unroll
  for (int s = 0; s < 4; ++s) ka[s] = L + kh * 8192 + q * 256 + (((g ^ q) << 4) ^ (s << 6));
  { const int row = 4 * g + (q >> 2), p = q & 3;
#pragma unroll
    for (int dt = 0; dt < 8; ++dt) va[dt] = L + kh * 8192 + row * 256 + ((dt ^ (4 * kh) ^ fsw(row)) << 5) + p * 8; }
  AttnS<NQ> A; f32x4 SA[NQ][2], SB[NQ][2];
#pragma unroll
  for (int t = 0; t < NQ; ++t) { A.Ls[t] = (f32x4){0.f, 0.f, 0.f, 0.f}; A.nm[t] = (f32x4){0.f, 0.f, 0.f, 0.f};
#pragma unroll
    for (int dt = 0; dt < 8; ++dt) A.O[t][dt] = (f32x4){0.f, 0.f, 0.f, 0.f}; }
  a_loadq<NQ>(A, L + QXOFF, qg, lane);
  vbar<12>();
#pragma unroll
  for (int t = 0; t < NQ; ++t) A.mx[t] = -3.0e38f;
#pragma unroll
  for (int kt = 0; kt < 2; ++kt) {
#pragma unroll
    for (int s = 0; s < 4; ++s) { const bf16x8 kf = ldsr(ka[s] + kt * 4096);
#pragma unroll
      for (int t = 0; t < NQ; ++t) SA[t][kt] = MFMA16(kf, A.Qf[t][s], s == 0 ? A.nm[t] : SA[t][kt]); }
#pragma unroll
    for (int t = 0; t < NQ; ++t) A.mx[t] = mx3(mx3(A.mx[t], SA[t][kt][0], SA[t][kt][1]), SA[t][kt][2], SA[t][kt][3]);
  }
  vlbar();
#pragma unroll
  for (int f = 0; f < 8; ++f) A.kf[f] = ldsr(ka[f & 3] + SLOT + (f >> 2) * 4096);
  a_check<NQ>(A, SA, true);
  const __amdgpu_buffer_rsrc_t ors = __builtin_amdgcn_make_buffer_rsrc(orow, 0, 0x7fffffff, 0x00020000);
#pragma unroll 1
#define AB_NIT 16
  for (int n0 = 0; n0 < AB_NIT; n0 += 2) {
    const int n = AB_NIT == 16 ? n0 : (n0 & 3);
    a_iter<NQ, 0>(A, SA, SB, ka, va, X, n, lane);
    a_check<NQ>(A, SB, false);
    f32x4 nmo[NQ];
#pragma unroll
    for (int t = 0; t < NQ; ++t) nmo[t] = A.nm[t];
    if (n == 6) {
      a_loadq<NQ>(A, L + Q1OFF, qg, lane);
#pragma unroll
      for (int t = 0; t < NQ; ++t) A.nm[t] = (f32x4){0.f, 0.f, 0.f, 0.f};
    }
    a_iter<NQ, 1>(A, SB, SA, ka, va, X, n + 1, lane);
    if ((n & 7) == 6) {
      if (n == 14) tail();
      LAS char* cb = L + (n == 6 ? 65536 : 0);
#pragma unroll
      for (int t = 0; t < NQ; ++t)
#pragma unroll
        for (int d = 0; d < 4; ++d) *(LAS f32x4*)(cb + wid * 8192 + (t * 4 + d) * 1024 + lane * 16) = A.O[t][4 + d];
      if (lane < 16) *(LAS f32x4*)(L + STOFF + wid * 256 + lane * 16) = (f32x4){nmo[0][0], A.Ls[0][0], nmo[NQ - 1][0], A.Ls[NQ - 1][0]};
      lbar();
      const f32x4 st = *(LAS const f32x4*)(L + STOFF + (wid ^ 4) * 256 + q * 16);
#pragma unroll
      for (int t = 0; t < NQ; ++t) {
        const float nmp = st[2 * t], lp = st[2 * t + 1], nmm = __builtin_fminf(nmo[t][0], nmp);
        float so = __builtin_amdgcn_exp2f(nmm - nmo[t][0]), sp = __builtin_amdgcn_exp2f(nmm - nmp);
        const float inv = __builtin_amdgcn_rcpf(A.Ls[t][0] * so + lp * sp); so *= inv; sp *= inv;
        f32x4 o[4];
#pragma unroll
        for (int d = 0; d < 4; ++d) o[d] = A.O[t][d] * so + *(LAS const f32x4*)(cb + (wid ^ 4) * 8192 + (t * 4 + d) * 1024 + lane * 16) * sp;
#pragma unroll
        for (int v = 0; v < 2; ++v)
          __builtin_amdgcn_raw_buffer_store_b128(pack32(o[2 * v], o[2 * v + 1]), ors, (unsigned)(((n >> 3) * 64 + 2 * kh + v) * XST + t * 1024 + lane * 16), 0, 16);
      }
    }
    if (n < 14) a_check<NQ>(A, SA, n == 6);
  }
}

struct HG { const char* w; const char* x; unsigned ldw, xstep; unsigned wv[4], xv[2]; unsigned wd, xd, xd1; };
DI void h_setup(HG& H, const char* x, unsigned ldx, const char* w0, const char* w1, unsigned ldw, unsigned lds0, int hw, int lane) {
  H.w = ((hw >> 1) ? w1 : w0) + (size_t)(16 * (hw & 1)) * ldw; H.ldw = ldw;
#pragma unroll
  for (int i = 0; i < 4; ++i) { const int row = 4 * i + (lane >> 4); H.wv[i] = row * ldw + (((lane & 15) ^ (fsw(row) << 1)) << 4); }
  if (ldx) {
    H.x = x + (size_t)(32 * hw) * ldx; H.xstep = 64;
    const unsigned xo = (lane >> 2) * ldx + (((lane & 3) ^ ((-(lane >> 4)) & 3)) << 4);
    H.xv[0] = xo; H.xv[1] = xo + (hw < 3 ? 16 * ldx : 0);
  } else {
    H.x = x + hw * 2048; H.xstep = XST; H.xv[0] = lane * 16; H.xv[1] = lane * 16 + (hw < 3 ? 1024 : 0);
  }
  H.wd = lds0 + WOFF + hw * 4096; H.xd = lds0 + XOFF + hw * 2048; H.xd1 = hw < 3 ? 1024 : 0;
}
template <int NT, int XP>
DI void h_issue(const HG& H, int kb  , int slot) {
  const char* w = H.w + (size_t)kb * 32 * H.ldw; const char* x = H.x + kb * H.xstep;
#pragma unroll
  for (int i = 0; i < 4; ++i) glds<NT>(w, H.wv[i], H.wd + slot * WST + i * 1024);
  glds<XP>(x, H.xv[0], H.xd + slot * XST); glds<XP>(x, H.xv[1], H.xd + slot * XST + H.xd1);
}

__global__ __launch_bounds__(512, 2) void fused(const char* xA, const char* kA, const char* vA, const char* wqA, const char* woA,
                                                const char* xB, const char* kB, const char* vB, const char* wqB, const char* woB, char* ws, char* out) {
  extern __shared__ __attribute__((aligned(16))) char lds[];
  LAS char* L = (LAS char*)lds; const unsigned lds0 = (unsigned)(uintptr_t)L;
  const int tid = threadIdx.x, lane = tid & 63; const int wid = __builtin_amdgcn_readfirstlane(tid >> 6);
  const int bid = blockIdx.x, xcd = bid & 7, idx = bid >> 3, br = xcd >> 2, j = xcd & 3, b = idx >> 2, c = idx & 3, h0 = 4 * j + c, h1 = h0 + 16;
  char* Ob = ws + WS_O + (size_t)(br * 8 + b) * (128 * XST);
  const int tile = (br * 8 + b) * 4 + c;
  const __amdgpu_buffer_rsrc_t prs = __builtin_amdgcn_make_buffer_rsrc(ws + WS_PART + (size_t)tile * PART_TILE, 0, (int)PART_TILE, 0x00020000);
  STAMP(0); CSTAMP(0);
  const int hw = wid & 3;
  const char* kc = (br ? kB : kA) + (size_t)(b * 32 + h0) * 131072; const char* vc = (br ? vB : vA) + (size_t)(b * 32 + h0) * 131072;
  GAS u64* fl = (GAS u64*)(ws + WS_FLAG);
  GAS u64* oflag = fl + 512 + ((br * 8 + b) * 4 + j) * 8; GAS u64* pflag = fl + 2048 + tile * 8;
  const int r32 = ROT ? (4 * b + 8 * c) & 31 : 0, r16 = ROT ? (2 * b + 4 * c) & 15 : 0;
  if (wid < 4) {
    f32x4 acc[7][4];
    c_gemm(acc, L, wid, lane, false);
    STAMP(1);
    lbar();
    { const float cs = 0.08838834764831845f * 1.4426950408889634f;
      LAS char* qi = L + ((wid >> 1) ? Q1OFF : QXOFF) + 2 * (wid & 1) * 1024 + lane * 16;
#pragma unroll
      for (int mt = 0; mt < 7; ++mt)
#pragma unroll
        for (int e = 0; e < 2; ++e) *(LAS u32x4*)(qi + (mt * 4 + e) * 1024) = pack32(acc[mt][2 * e] * cs, acc[mt][2 * e + 1] * cs); }
    lbar();
  } else {
    const char* x = br ? xB : xA; const char* wq = br ? wqB : wqA;
    HG H; h_setup(H, x + (size_t)b * 112 * 2048, 2048, wq + h0 * 256, wq + h1 * 256, 8192, lds0, hw, lane);
#pragma unroll
    for (int s = 0; s < 4; ++s) h_issue<W_NT, 0>(H, (s + r32) & 31, s);
    HA A; h_kv_setup(A, kc, vc, 16 * hw, lds0, lane);
    vbar<18>();
#pragma unroll 1
    for (int s = 0; s < 28; ++s) { vbar<12>(); if ((s & 3) == 0) HSTAMP(s >> 2); h_issue<W_NT2, 0>(H, (s + 4 + r32) & 31, s & 3); }
    vbar<12>(); h_kv<1>(A, 0);
    vbar<10>(); h_kv<1>(A, 1);
    vbar<8>(); h_kv<1>(A, 2);
    bar(); h_kv<1>(A, 3);
    bar(); bar();
  }
  STAMP(2);
  u64 ep = 0;
  if (wid >= 4) { u64 seed; asm volatile("s_load_dwordx2 %0, %1, 0x0\n\ts_waitcnt lgkmcnt(0)" : "=s"(seed) : "s"(fl) : "memory"); ep = mix64(seed); }
  const char* wo = (br ? woB : woA) + (size_t)(512 * j) * 2048 + c * 512;
  { AttnX X; X.oflag = oflag; X.ep = ep; X.c = c; X.w7 = wid == 7;
    auto tail = [&]() { if (wid >= 4) {
        int hz = hw, lz = lane; asm volatile("" : "+s"(hz)); asm volatile("" : "+v"(lz));
        HG H; h_setup(H, Ob + j * 16 * XST, 0, wo, wo + 256, 2048, lds0, hz, lz);
        wait_flags4(oflag, ep, lz);
#pragma unroll 1
        for (int s = 0; s < 4; ++s) h_issue<W_NT, 2>(H, (s + r16) & 15, s); } };
    char* orow = Ob + h0 * 4 * XST + 2 * hw * 1024;
    if (hw < 3) c_attn<2>(L, wid, lane, orow, X, tail);
    else { h_kv_setup(X.H, kc, vc, 32 * (wid >> 2), lds0, lane); c_attn<1>(L, wid, lane, orow, X, tail); } }
  STAMP(4);
  if (wid < 4) {
    f32x4 acc[7][4];
    c_gemm(acc, L, wid, lane, true);
    STAMP(5);
    lbar();
    if (wid != j) {
#pragma unroll
      for (int mt = 0; mt < 7; ++mt)
#pragma unroll
        for (int e = 0; e < 4; ++e)
          __builtin_amdgcn_raw_buffer_store_b128(__builtin_bit_cast(u32x4, acc[mt][e]), prs, lane * 16 + ((wid * 4 + j) * 28 + mt * 4 + e) * 1024, 0, 16);
    } else {
#pragma unroll
      for (int mt = 0; mt < 7; ++mt)
#pragma unroll
        for (int e = 0; e < 4; ++e) *(LAS f32x4*)(L + QXOFF + (mt * 4 + e) * 1024 + lane * 16) = acc[mt][e];
    }
    vlbar();
    STAMP(6);
    bar();
    STAMP(7);
  } else {
    int hz = hw, lz = lane; asm volatile("" : "+s"(hz)); asm volatile("" : "+v"(lz));
    HG H; h_setup(H, Ob + j * 16 * XST, 0, wo, wo + 256, 2048, lds0, hz, lz);
    u64 fv = 0;
    vbar<18>();
#pragma unroll 1
    for (int s = 0; s < 28; ++s) {
      vbar<12>();
      if (s == 9) asm volatile("global_load_dwordx2 %0, %1, %2 sc1" : "=v"(fv) : "v"((lz & 3) * 8 + 32), "s"(oflag) : "memory");
      if (s == 12) { asm volatile("" : "+v"(fv)); if (!__all(fv == ep)) wait_flags4(oflag + 4, ep, lz); }
      const int n = s + 4, kb = (n >> 4) * 64 + ((n + r16) & 15);
      if ((n & 15) < 2) h_issue<W_NT, 2>(H, kb, s & 3); else h_issue<W_NT2, 2>(H, kb, s & 3);
      if (s == 3 && tid == 256) __hip_atomic_store(oflag + 4 + c, ep, RLX_AGENT);
    }
    vbar<12>(); vbar<6>(); vbar<0>(); bar();
    bar();
    bar();
    if (hw == 0) { if (lane == 0) __hip_atomic_store(pflag + j, ep, RLX_AGENT); wait_flags4(pflag, ep, lane); }
    vbar<0>();
    if (bid == 0 && hw == 3) {
#pragma unroll 1
      for (unsigned spins = 0; spins < (1u << 17); ++spins) {
        bool ok = true;
#pragma unroll
        for (int k = 0; k < 4; ++k) ok &= __hip_atomic_load(fl + 512 + lane * 8 + 4 + k, RLX_AGENT) == ep;
        if (__all(ok)) break;
        __builtin_amdgcn_s_sleep(2);
      }
      if (lane == 0) __hip_atomic_store(fl, ep, RLX_AGENT);
    }
  }
  if (wid < 7) {
    f32x4 in[3][4];
#pragma unroll
    for (int k = 0; k < 3; ++k) { const int src = (j + 1 + k) & 3;
#pragma unroll
      for (int e = 0; e < 4; ++e)
        in[k][e] = __builtin_bit_cast(f32x4, __builtin_amdgcn_raw_buffer_load_b128(prs, lane * 16, ((j * 4 + src) * 28 + wid * 4 + e) * 1024, 16)); }
    const int q = lane & 15, g = lane >> 4;
    f32x4 sum[4];
#pragma unroll
    for (int e = 0; e < 4; ++e) sum[e] = *(LAS const f32x4*)(L + QXOFF + (wid * 4 + e) * 1024 + lane * 16) + in[0][e] + (in[1][e] + in[2][e]);
#pragma unroll
    for (int pr = 0; pr < 2; ++pr)
      *(GAS u32x4*)(out + ((size_t)(b * 112 + 16 * wid + q) * 2048 + br * 1024 + c * 256 + j * 64 + pr * 32 + g * 8) * 2) = pack32(sum[2 * pr], sum[2 * pr + 1]);
  }
  STAMP(8); CSTAMP(1);
}

extern "C" void kernel_launch(void* const* d_in, const int* in_sizes, int n_in, void* d_out, int out_size, void* d_ws, size_t ws_size, hipStream_t stream) {
  (void)in_sizes; (void)n_in; (void)out_size; (void)ws_size;
  (void)hipFuncSetAttribute((const void*)fused, hipFuncAttributeMaxDynamicSharedMemorySize, LDS_BYTES);
  fused<<<256, 512, LDS_BYTES, stream>>>((const char*)d_in[0], (const char*)d_in[1], (const char*)d_in[2], (const char*)d_in[3], (const char*)d_in[4],
                                         (const char*)d_in[5], (const char*)d_in[6], (const char*)d_in[7], (const char*)d_in[8], (const char*)d_in[9], (char*)d_ws, (char*)d_out);
}
```

```cpp
#include <hip/hip_runtime.h>
#include <hip/hip_bf16.h>
#include <stdint.h>

typedef __attribute__((ext_vector_type(8))) short bf16x8;
typedef __attribute__((ext_vector_type(4))) short s16x4;
typedef __attribute__((ext_vector_type(4))) float f32x4;
typedef __attribute__((ext_vector_type(2))) float f32x2;
typedef __attribute__((ext_vector_type(16))) float f32x16;
typedef __attribute__((ext_vector_type(4))) unsigned u32x4;
typedef __attribute__((ext_vector_type(2))) __bf16 bf16x2_t;
typedef short v4i16_t __attribute__((ext_vector_type(4)));
typedef unsigned long long u64;
#define LAS __attribute__((address_space(3)))
#define GAS __attribute__((address_space(1)))
#define DI __device__ __forceinline__
#define MFMA16(a, b, c) __builtin_amdgcn_mfma_f32_16x16x32_bf16((a), (b), (c), 0, 0, 0)
#define MFMA32(a, b, c) __builtin_amdgcn_mfma_f32_32x32x16_bf16((a), (b), (c), 0, 0, 0)
#define SBAR() __builtin_amdgcn_sched_barrier(0)
#define RLX_AGENT __ATOMIC_RELAXED, __HIP_MEMORY_SCOPE_AGENT

#define W_NT 0
#define W_NT2 0
#define RB 8
#define PBF16 1
#define STAGGER 1
#define PF 0
#define PF_NT 0
#define X_NT 0
#define X_NT2 0
#define KV_NT 1
#define NTN 2
#define ROT 1
#define THR 8.0f

constexpr int LDS_BYTES = 163840;
constexpr int SLOT = 16384;
constexpr int WOFF = 65536, WST = 16384;
constexpr int XOFF = 131072, XST = 7168;
constexpr int QXOFF = 65536;
constexpr int Q1OFF = 131072;
constexpr int Q0OFF = 98304;
constexpr int OS0OFF = Q0OFF, OS1OFF = 2 * SLOT;
constexpr int PFOFF = 159744;
constexpr int NSL = 6;
constexpr size_t WS_O = 0;
constexpr size_t WS_PART = 16u << 20;
constexpr size_t PART_TILE = 16 * 28 * 1024;
constexpr size_t WS_FLAG = 64u << 20;

template <int NT>
DI void glds(const void* sbase, unsigned voff, unsigned ldsdst) {
  if (NT == 1) asm volatile("s_mov_b32 m0, %2\n\ts_nop 3\n\tglobal_load_lds_dwordx4 %0, %1 nt" ::"v"(voff), "s"(sbase), "s"(ldsdst) : "memory");
  else if (NT == 2) asm volatile("s_mov_b32 m0, %2\n\ts_nop 3\n\tglobal_load_lds_dwordx4 %0, %1 sc1" ::"v"(voff), "s"(sbase), "s"(ldsdst) : "memory");
  else asm volatile("s_mov_b32 m0, %2\n\ts_nop 3\n\tglobal_load_lds_dwordx4 %0, %1" ::"v"(voff), "s"(sbase), "s"(ldsdst) : "memory");
}
template <int N> DI void vbar() { asm volatile("s_waitcnt vmcnt(%0)\n\ts_barrier" ::"i"(N) : "memory"); }
template <int N> DI void wait_vm() { asm volatile("s_waitcnt vmcnt(%0)" ::"i"(N) : "memory"); }
DI void lbar() { asm volatile("s_waitcnt lgkmcnt(0)\n\ts_barrier" ::: "memory"); }
DI void bar() { asm volatile("s_barrier" ::: "memory"); }
DI unsigned cvtpk(float lo, float hi) { const f32x2 v = {lo, hi}; return __builtin_bit_cast(unsigned, __builtin_convertvector(v, bf16x2_t)); }
#define STAMP(i) do {} while (0)
#define CSTAMP(i) do {} while (0)
#define HSTAMP(i) do {} while (0)
DI s16x4 trread(LAS const char* p) { return __builtin_bit_cast(s16x4, __builtin_amdgcn_ds_read_tr16_b64_v4i16((LAS v4i16_t*)p)); }
DI bf16x8 ldsr(LAS const char* p) { return *(LAS const bf16x8*)p; }
DI bf16x8 cat8(s16x4 a, s16x4 b) { return __builtin_shufflevector(a, b, 0, 1, 2, 3, 4, 5, 6, 7); }
DI int fsw(int row) { return (row & 3) | ((((row >> 2) ^ (row >> 3)) & 1) << 2); }
DI u64 mix64(u64 z) { z += 0x9E3779B97F4A7C15ull; z = (z ^ (z >> 30)) * 0xBF58476D1CE4E5B9ull; z = (z ^ (z >> 27)) * 0x94D049BB133111EBull; return z ^ (z >> 31); }

DI u32x4 pack32(f32x4 a, f32x4 b) {
  auto r0 = __builtin_amdgcn_permlane32_swap(cvtpk(a[0], a[1]), cvtpk(b[0], b[1]), false, false);
  auto r1 = __builtin_amdgcn_permlane32_swap(cvtpk(a[2], a[3]), cvtpk(b[2], b[3]), false, false);
  auto s0 = __builtin_amdgcn_permlane16_swap(r0[0], r0[1], false, false);
  auto s1 = __builtin_amdgcn_permlane16_swap(r1[0], r1[1], false, false);
  u32x4 o = {s0[0], s1[0], s0[1], s1[1]};
  return o;
}
DI float rowmax4(float mx) {
  auto r = __builtin_amdgcn_permlane32_swap(__float_as_uint(mx), __float_as_uint(mx), false, false);
  mx = __builtin_fmaxf(__uint_as_float(r[0]), __uint_as_float(r[1]));
  auto r2 = __builtin_amdgcn_permlane16_swap(__float_as_uint(mx), __float_as_uint(mx), false, false);
  return __builtin_fmaxf(__uint_as_float(r2[0]), __uint_as_float(r2[1]));
}
DI float max4(float m, const f32x4& c) { return __builtin_fmaxf(__builtin_fmaxf(__builtin_fmaxf(m, c[0]), c[1]), __builtin_fmaxf(c[2], c[3])); }

template <bool VM>
DI void c_stage(f32x4 (&acc)[7][4], bf16x8 (&X)[7], const bf16x8 (&W)[4], bf16x8 (&Wn)[4], LAS const char* xr, LAS const char* const (&wr)[8], int cs, int ns) {
  s16x4 lo[4];
#pragma unroll
  for (int gp = 0; gp < 28; ++gp) {
    const int mt = gp >> 2, e = gp & 3;
    acc[mt][e] = MFMA16(W[e], X[mt], acc[mt][e]);
    SBAR();
    if (gp == 0) X[6] = ldsr(xr + cs * XST + 6 * 1024);
    if (gp == 5) { if (VM) asm volatile("s_waitcnt vmcnt(0) lgkmcnt(0)\n\ts_barrier" ::: "memory"); else lbar(); }
    if (gp >= 6 && gp < 22 && (gp & 1) == 0) { const int k = (gp - 6) >> 1;
      if ((k & 1) == 0) lo[k >> 1] = trread(wr[k >> 1] + ns * WST); else Wn[k >> 1] = cat8(lo[k >> 1], trread(wr[4 + (k >> 1)] + ns * WST + 1024)); }
    if (gp >= 7 && (gp & 3) == 3) X[mt - 1] = ldsr(xr + ns * XST + (mt - 1) * 1024);
    SBAR();
  }
}
DI void c_gemm(f32x4 (&acc)[7][4], LAS char* L, int wid, int lane, bool xlin) {
  asm volatile("" : "+v"(lane));
  const int q = lane & 15, g = lane >> 4;
  LAS const char* xr = L + XOFF + (xlin ? lane * 16 : q * 64 + ((g ^ ((-(q >> 2)) & 3)) << 4));
  LAS const char* wr[8];
  { const int row = 8 * g + (q >> 2), p = q & 3, f = fsw(row), nt0 = 4 * (wid & 1);
    LAS const char* wb = L + WOFF + (wid >> 1) * 8192 + row * 256 + p * 8;
#pragma unroll
    for (int e = 0; e < 4; ++e) { wr[e] = wb + (((nt0 + e) ^ f) << 5); wr[4 + e] = wb + (((nt0 + e) ^ f ^ 4) << 5); } }
#pragma unroll
  for (int mt = 0; mt < 7; ++mt)
#pragma unroll
    for (int e = 0; e < 4; ++e) acc[mt][e] = (f32x4){0.f, 0.f, 0.f, 0.f};
  bf16x8 X[7], Wa[4], Wb[4];
  lbar();
#pragma unroll
  for (int e = 0; e < 4; ++e) Wa[e] = cat8(trread(wr[e]), trread(wr[4 + e] + 1024));
#pragma unroll
  for (int mt = 0; mt < 6; ++mt) X[mt] = ldsr(xr + mt * 1024);
#pragma unroll 1
  for (int s = 0; s < 32; s += 4) {
    c_stage<false>(acc, X, Wa, Wb, xr, wr, 0, 1);
    c_stage<false>(acc, X, Wb, Wa, xr, wr, 1, 2);
    c_stage<false>(acc, X, Wa, Wb, xr, wr, 2, 3);
    c_stage<true>(acc, X, Wb, Wa, xr, wr, 3, 0);
  }
}

constexpr int DST = 30720;
DI void c_stage128(f32x4 (&acc)[7][2], bf16x8 (&X)[14], const bf16x8 (&W)[4], bf16x8 (&Wn)[4], LAS const char*& xr, LAS const char* (&wr)[4], int dn) {
  s16x4 lo[4];
#pragma unroll
  for (int gp = 0; gp < 28; ++gp) {
    const int i = gp >> 1, e = gp & 1, kh = i / 7, mt = i % 7;
    acc[mt][e] = MFMA16(W[2 * kh + e], X[i], acc[mt][e]);
    SBAR();
    if (gp < 3) X[11 + gp] = ldsr(xr + (11 + gp) * 1024);
    if (gp == 3) { xr += dn; wr[0] += dn; wr[1] += dn; }
    if (gp == 4) { wr[2] += dn; wr[3] += dn; }
    if (gp == 6) lbar();
    if (gp >= 7 && (gp & 1) == 1) { const int z = (gp - 7) >> 1; X[z] = ldsr(xr + z * 1024); }
    if (gp >= 8 && gp < 24 && (gp & 1) == 0) { const int k = (gp - 8) >> 1, f = k >> 1;
      if ((k & 1) == 0) lo[f] = trread(wr[f & 1] + (f >> 1) * 8192); else Wn[f] = cat8(lo[f], trread(wr[2 + (f & 1)] + (f >> 1) * 8192 + 1024)); }
    SBAR();
  }
}
DI void c_gemm128(f32x4 (&acc)[7][2], LAS char* L, int wid, int lane) {
  asm volatile("" : "+v"(lane));
  const int q = lane & 15, g = lane >> 4;
  LAS const char* xr = L + WOFF + 16384 + q * 64 + ((g ^ ((-(q >> 2)) & 3)) << 4);
  LAS const char* wr[4];
  { const int row = 8 * g + (q >> 2), p = q & 3, f = fsw(row), nt0 = 2 * wid;
    LAS const char* wb = L + WOFF + row * 256 + p * 8;
#pragma unroll
    for (int e = 0; e < 2; ++e) { wr[e] = wb + (((nt0 + e) ^ f) << 5); wr[2 + e] = wb + (((nt0 + e) ^ f ^ 4) << 5); } }
#pragma unroll
  for (int mt = 0; mt < 7; ++mt) { acc[mt][0] = (f32x4){0.f, 0.f, 0.f, 0.f}; acc[mt][1] = (f32x4){0.f, 0.f, 0.f, 0.f}; }
  bf16x8 X[14], Wa[4], Wb[4];
  lbar();
#pragma unroll
  for (int f = 0; f < 4; ++f) Wa[f] = cat8(trread(wr[f & 1] + (f >> 1) * 8192), trread(wr[2 + (f & 1)] + (f >> 1) * 8192 + 1024));
#pragma unroll
  for (int i = 0; i < 11; ++i) X[i] = ldsr(xr + i * 1024);
  int sl = 0;
#pragma unroll 1
  for (int s = 0; s < 16; s += 2) {
    c_stage128(acc, X, Wa, Wb, xr, wr, sl == 2 ? -2 * DST : DST); sl = sl == 2 ? 0 : sl + 1;
    c_stage128(acc, X, Wb, Wa, xr, wr, sl == 2 ? -2 * DST : DST); sl = sl == 2 ? 0 : sl + 1;
  }
}

#define PIN(x) asm volatile("" : "+v"(x))
DI float mx3(float a, float b, float c) { return __builtin_elementwise_maximum(__builtin_elementwise_maximum(a, b), c); }
DI void vlbar() { asm volatile("s_waitcnt vmcnt(0) lgkmcnt(0)\n\ts_barrier" ::: "memory"); }

struct HA { const char* k0; const char* v0; unsigned kv[4], vv[4]; unsigned d; };
DI void h_kv(const HA& A, int T) {
  const int n = T >> 1; const unsigned so = (unsigned)(n >> 3) * (16u * 131072u) + (n & 7) * SLOT;
  const unsigned d = A.d + (T % NSL) * SLOT;
  if (T & 1) {
#pragma unroll
    for (int i = 0; i < 4; ++i) glds<KV_NT>(A.v0 + so, A.vv[i], d + i * 1024);
  } else {
#pragma unroll
    for (int i = 0; i < 4; ++i) glds<KV_NT>(A.k0 + so, A.kv[i], d + i * 1024);
  }
}
DI void h_kv_setup(HA& A, const char* k0, const char* v0, int row0  , unsigned lds0, int lane) {
  A.k0 = k0 + row0 * 256; A.v0 = v0 + row0 * 256; A.d = lds0 + row0 * 256;
#pragma unroll
  for (int i = 0; i < 4; ++i) { const int row = 4 * i + (lane >> 4); A.kv[i] = row * 256 + (((lane & 15) ^ row) << 4); A.vv[i] = row * 256 + (((lane & 15) ^ ((row & 3) << 2)) << 4); }
}
DI void h_ostore(LAS const char* src  , char* dst, int lane) {
  const __amdgpu_buffer_rsrc_t ors = __builtin_amdgcn_make_buffer_rsrc(dst, 0, XST, 0x00020000);
  u32x4 v[7];
#pragma unroll
  for (int i = 0; i < 7; ++i) v[i] = *(LAS const u32x4*)(src + i * 1024 + lane * 16);
#pragma unroll
  for (int i = 0; i < 7; ++i) __builtin_amdgcn_raw_buffer_store_b128(v[i], ors, i * 1024 + lane * 16, 0, 16);
}
DI void wait_flags4(GAS u64* f, u64 e, int lane) {
#pragma unroll 1
  for (unsigned spins = 0; spins < (1u << 17); ++spins) {
    const u64 v = __hip_atomic_load(f + (lane & 3), RLX_AGENT);
    if (__all(v == e)) break;
    __builtin_amdgcn_s_sleep(1);
  }
}
DI float xhalf_max(float m) { auto r = __builtin_amdgcn_permlane32_swap(__float_as_uint(m), __float_as_uint(m), false, false); return __builtin_fmaxf(__uint_as_float(r[0]), __uint_as_float(r[1])); }
DI float xhalf_sum(float m) { auto r = __builtin_amdgcn_permlane32_swap(__float_as_uint(m), __float_as_uint(m), false, false); return __uint_as_float(r[0]) + __uint_as_float(r[1]); }
struct AttnS { f32x16 O[4], nm, Ls; bf16x8 Qf[8], kf[8]; float mx; bool big; };
#define LGKM(n) __builtin_amdgcn_s_waitcnt(0xC07F | ((n) << 8))

template <int GK>
DI void a_grp(AttnS& A, f32x16& So, f32x16& Sn, LAS const char* const (&ka)[8], LAS const char* const (&va)[4]) {
  const bf16x8 ones = {0x3F80, 0x3F80, 0x3F80, 0x3F80, 0x3F80, 0x3F80, 0x3F80, 0x3F80};
  u32x4 pw[2]; s16x4 vl[8], vh[8];
#pragma unroll
  for (int sl = 0; sl < 18; ++sl) {
    if (sl == 0) LGKM(4);
    if (sl == 4 && GK == 1) LGKM(6);
    if (sl == 9) LGKM(8);
    if (sl == 14) LGKM(5);
    if (sl < 8) Sn = MFMA32(A.kf[sl], A.Qf[sl], sl == 0 ? A.nm : Sn);
    else { const int u = (sl - 8) / 5, r = (sl - 8) % 5; const bf16x8 pf = __builtin_bit_cast(bf16x8, pw[u]);
      if (r == 0) A.Ls = MFMA32(ones, pf, A.Ls); else A.O[r - 1] = MFMA32(cat8(vl[4 * u + r - 1], vh[4 * u + r - 1]), pf, A.O[r - 1]); }
    SBAR();
    { const int e0 = (sl * 4 + 2) / 3, e1 = (sl * 4 + 6) / 3;
#pragma unroll
      for (int e = (sl == 0 ? 0 : e0); e < e1; ++e) if (e < 16) {
        float x = __builtin_amdgcn_exp2f(So[e]); PIN(x); So[e] = x;
      }
#pragma unroll
      for (int p = 0; p < 8; ++p) if (sl >= 1 && 2 * p + 2 <= e0 && 2 * p + 2 > ((sl - 1) * 4 + 2) / 3 - (sl == 1 ? 1 : 0)) {
        unsigned w = cvtpk(So[2 * p], So[2 * p + 1]); PIN(w); pw[p >> 2][p & 3] = w; } }
    if (GK == 0 && sl == 0) { PIN(Sn); LGKM(0); bar(); }
    if (sl >= 1 && sl <= 8) { const int z = sl - 1;
      vl[z] = trread(va[z & 3] + GK * 8192 + (z >> 2) * 4096); vh[z] = trread(va[z & 3] + GK * 8192 + (z >> 2) * 4096 + 2048);
    }
    if (sl >= 9 && sl <= 16) {
      A.kf[sl - 9] = ldsr(ka[sl - 9] + GK * 8192);
    }
    if (sl >= 12 && sl <= 15) { const int z = sl - 12;
      float x = mx3(z == 0 ? Sn[0] : A.mx, Sn[4 * z], Sn[4 * z + 1]); x = mx3(x, Sn[4 * z + 2], Sn[4 * z + 3]); PIN(x); A.mx = x; }
    if (sl == 16) A.big = __any(A.mx > THR);
    SBAR();
  }
}
DI void a_check(AttnS& A, f32x16& Sn, bool first) {
  if (first || A.big) {
    float d = xhalf_max(A.mx);
    if (!first) d = __builtin_fmaxf(d, 0.f);
    const float f = first ? 0.f : __builtin_amdgcn_exp2f(-d);
    A.nm -= d; Sn -= d;
#pragma unroll
    for (int dt = 0; dt < 4; ++dt) A.O[dt] *= f;
    A.Ls *= f;
  }
}
DI void a_loadq(AttnS& A, LAS const char* Qi, int wid, int lane) {
  const int q = lane & 31, h = lane >> 5; int mt = 2 * wid + (q >> 4); mt = mt > 6 ? 6 : mt;
  LAS const char* p = Qi + mt * 4096 + (h * 16 + (q & 15)) * 16;
#pragma unroll
  for (int s = 0; s < 8; ++s) A.Qf[s] = ldsr(p + (s >> 1) * 1024 + (s & 1) * 512);
#pragma unroll
  for (int i = 0; i < 16; ++i) A.nm[i] = 0.f;
}
DI void c_attn(LAS char* L, int wid, int lane, int nper  ) {
  asm volatile("" : "+v"(lane));
  const int r = lane & 31, h = lane >> 5;
  LAS const char* ka[8]; LAS const char* va[4];
#pragma unroll
  for (int s = 0; s < 8; ++s) ka[s] = L + r * 256 + (((h ^ (r & 15)) << 4) ^ (s << 5));
  { const int db = (lane >> 4) & 1, qq = (lane & 15) >> 2, p = lane & 3;
#pragma unroll
    for (int dt = 0; dt < 4; ++dt) va[dt] = L + SLOT + (4 * h + qq) * 256 + (((2 * dt + db) ^ (qq << 1)) << 5) + p * 8; }
  AttnS A; f32x16 SA, SB;
#pragma unroll
  for (int dt = 0; dt < 4; ++dt)
#pragma unroll
    for (int i = 0; i < 16; ++i) A.O[dt][i] = 0.f;
#pragma unroll
  for (int i = 0; i < 16; ++i) A.Ls[i] = 0.f;
  a_loadq(A, L + Q0OFF, wid, lane);
  bar();
#pragma unroll
  for (int s = 0; s < 8; ++s) SA = MFMA32(ldsr(ka[s]), A.Qf[s], s == 0 ? A.nm : SA);
#pragma unroll
  for (int s = 0; s < 8; ++s) A.kf[s] = ldsr(ka[s] + 8192);
  A.mx = SA[0];
#pragma unroll
  for (int i = 0; i < 16; i += 2) A.mx = mx3(A.mx, SA[i], SA[i + 1]);
  a_check(A, SA, true);
  int ks = 0;
#pragma unroll 1
  for (int n = 0; n < nper; ++n) {
    const int dk = ks == 2 ? -4 * SLOT : 2 * SLOT;
#pragma unroll
    for (int s = 0; s < 8; ++s) ka[s] += dk;
    a_grp<0>(A, SA, SB, ka, va);
    a_check(A, SB, false);
    if (n == 7) a_loadq(A, L + Q1OFF, wid, lane);
    a_grp<1>(A, SB, SA, ka, va);
    if ((n & 7) == 7) {
      const float inv = __builtin_amdgcn_rcpf(A.Ls[0]);
      LAS char* os = L + (nper == 8 ? 0 : (n == 7 ? OS0OFF : OS1OFF)) + 2 * wid * 1024 + ((r >> 4) * 64 + h * 16 + (r & 15)) * 16;
      if (wid < 3 || r < 16) {
#pragma unroll
        for (int dt = 0; dt < 4; ++dt)
#pragma unroll
          for (int g = 0; g < 4; g += 2) {
            auto x0 = __builtin_amdgcn_permlane32_swap(cvtpk(A.O[dt][4 * g] * inv, A.O[dt][4 * g + 1] * inv), cvtpk(A.O[dt][4 * g + 4] * inv, A.O[dt][4 * g + 5] * inv), false, false);
            auto x1 = __builtin_amdgcn_permlane32_swap(cvtpk(A.O[dt][4 * g + 2] * inv, A.O[dt][4 * g + 3] * inv), cvtpk(A.O[dt][4 * g + 6] * inv, A.O[dt][4 * g + 7] * inv), false, false);
            const u32x4 o = {x0[0], x1[0], x0[1], x1[1]};
            *(LAS u32x4*)(os + dt * XST + g * 256) = o;
          }
      }
    }
    if (n < nper - 1) a_check(A, SA, n == 7);
#pragma unroll
    for (int dt = 0; dt < 4; ++dt) va[dt] += dk;
    ks = ks == 2 ? 0 : ks + 1;
  }
}

struct HG { const char* w; const char* x; unsigned ldw, xstep; unsigned wv[4], xv[2]; unsigned wd, xd, xd1; };
DI void h_setup(HG& H, const char* x, unsigned ldx, const char* w0, const char* w1, unsigned ldw, unsigned lds0, int hw, int lane) {
  H.w = ((hw >> 1) ? w1 : w0) + (size_t)(16 * (hw & 1)) * ldw; H.ldw = ldw;
#pragma unroll
  for (int i = 0; i < 4; ++i) { const int row = 4 * i + (lane >> 4); H.wv[i] = row * ldw + (((lane & 15) ^ (fsw(row) << 1)) << 4); }
  if (ldx) {
    H.x = x + (size_t)(32 * hw) * ldx; H.xstep = 64;
    const unsigned xo = (lane >> 2) * ldx + (((lane & 3) ^ ((-(lane >> 4)) & 3)) << 4);
    H.xv[0] = xo; H.xv[1] = xo + (hw < 3 ? 16 * ldx : 0);
  } else {
    H.x = x + hw * 2048; H.xstep = XST; H.xv[0] = lane * 16; H.xv[1] = lane * 16 + (hw < 3 ? 1024 : 0);
  }
  H.wd = lds0 + WOFF + hw * 4096; H.xd = lds0 + XOFF + hw * 2048; H.xd1 = hw < 3 ? 1024 : 0;
}
template <int NT, int XP>
DI void h_issue(const HG& H, int kb  , int slot) {
  const char* w = H.w + (size_t)kb * 32 * H.ldw; const char* x = H.x + kb * H.xstep;
#pragma unroll
  for (int i = 0; i < 4; ++i) glds<NT>(w, H.wv[i], H.wd + slot * WST + i * 1024);
  glds<XP>(x, H.xv[0], H.xd + slot * XST); glds<XP>(x, H.xv[1], H.xd + slot * XST + H.xd1);
}
struct HG2 { const char* w; const char* x; unsigned wv[4], xv[4]; unsigned wd, xd, xd3; };
DI void h_setup128(HG2& H, const char* x, const char* w, unsigned lds0, int hw, int lane) {
  H.w = w + (size_t)(16 * hw) * 8192;
#pragma unroll
  for (int i = 0; i < 4; ++i) { const int row = 4 * i + (lane >> 4); H.wv[i] = row * 8192 + (((lane & 15) ^ (fsw(row) << 1)) << 4); }
  H.x = x + (size_t)(64 * (hw & 1)) * 2048 + (hw >> 1) * 64;
  const unsigned xo = (lane >> 2) * 2048 + (((lane & 3) ^ ((-(lane >> 4)) & 3)) << 4);
#pragma unroll
  for (int i = 0; i < 4; ++i) H.xv[i] = xo + ((i == 3 && (hw & 1)) ? 2 : i) * 16 * 2048;
  H.wd = lds0 + WOFF + hw * 4096; H.xd = lds0 + WOFF + 16384 + (hw >> 1) * XST + (hw & 1) * 4096; H.xd3 = (hw & 1) ? 2048 : 3072;
}
template <int NT, int XP>
DI void h_issue128(const HG2& H, int kd  , int slot) {
  const char* w = H.w + (size_t)kd * 64 * 8192; const char* x = H.x + kd * 128;
#pragma unroll
  for (int i = 0; i < 4; ++i) glds<NT>(w, H.wv[i], H.wd + slot * DST + i * 1024);
#pragma unroll
  for (int i = 0; i < 4; ++i) glds<XP>(x, H.xv[i], H.xd + slot * DST + (i == 3 ? H.xd3 : i * 1024));
}

__global__ __launch_bounds__(512, 2) void fused(const char* xA, const char* kA, const char* vA, const char* wqA, const char* woA,
                                                const char* xB, const char* kB, const char* vB, const char* wqB, const char* woB, char* ws, char* out) {
  extern __shared__ __attribute__((aligned(16))) char lds[];
  LAS char* L = (LAS char*)lds; const unsigned lds0 = (unsigned)(uintptr_t)L;
  const int tid = threadIdx.x, lane = tid & 63; const int wid = __builtin_amdgcn_readfirstlane(tid >> 6);
  const int bid = blockIdx.x, xcd = bid & 7, idx = bid >> 3, br = xcd >> 2, j = xcd & 3, b = idx >> 2, c = idx & 3, h0 = 4 * j + c, h1 = h0 + 16;
  char* Ob = ws + WS_O + (size_t)(br * 8 + b) * (128 * XST);
  const int tile = (br * 8 + b) * 4 + c;
  const __amdgpu_buffer_rsrc_t prs = __builtin_amdgcn_make_buffer_rsrc(ws + WS_PART + (size_t)tile * PART_TILE, 0, (int)PART_TILE, 0x00020000);
  STAMP(0); CSTAMP(0);
  const int hw = wid & 3;
  const char* kc = (br ? kB : kA) + (size_t)(b * 32 + h0) * 131072; const char* vc = (br ? vB : vA) + (size_t)(b * 32 + h0) * 131072;
  GAS u64* fl = (GAS u64*)(ws + WS_FLAG);
  GAS u64* oflag = fl + 512 + ((br * 8 + b) * 4 + j) * 8; GAS u64* pflag = fl + 2048 + tile * 8;
  const int r8 = RB == 16 ? (2 * b + 4 * c) & 15 : RB == 8 ? (b + 2 * c) & 7 : (b + c) & (RB - 1);
#define KROT(s) (((s) & ~(RB - 1)) | (((s) + r8) & (RB - 1)))
  u64 ep = 0;
  const char* wo = (br ? woB : woA) + (size_t)(512 * j) * 2048 + c * 512;
  const bool grpB = STAGGER && (b & 1);
  const float cs = 0.08838834764831845f * 1.4426950408889634f;
  if (wid < 4) {
#pragma unroll 1
    for (int ph = 0; ph < (grpB ? 2 : 1); ++ph) {
      int lz = lane; asm volatile("" : "+v"(lz));
      if (grpB) {
        f32x4 acc[7][2];
        c_gemm128(acc, L, wid, lane);
        lbar();
#pragma unroll
        for (int mt = 0; mt < 7; ++mt) *(LAS u32x4*)(L + Q0OFF + (mt * 4 + wid) * 1024 + lz * 16) = pack32(acc[mt][0] * cs, acc[mt][1] * cs);
      } else {
        f32x4 acc[7][4];
        c_gemm(acc, L, wid, lane, false);
        lbar();
        LAS char* qi = L + ((wid >> 1) ? Q1OFF : Q0OFF) + 2 * (wid & 1) * 1024 + lz * 16;
#pragma unroll
        for (int mt = 0; mt < 7; ++mt)
#pragma unroll
          for (int e = 0; e < 2; ++e) *(LAS u32x4*)(qi + (mt * 4 + e) * 1024) = pack32(acc[mt][2 * e] * cs, acc[mt][2 * e + 1] * cs);
      }
      lbar();
      STAMP(1 + ph);
      c_attn(L, wid, lane, grpB ? 8 : 16);
      if (ph == 0) STAMP(3);
    }
    STAMP(4);
  } else {
    const char* x = (br ? xB : xA) + (size_t)b * 112 * 2048; const char* wq = br ? wqB : wqA;
    HA A; h_kv_setup(A, kc, vc, 16 * hw, lds0, lane);
    if (!grpB) {
      HG H; h_setup(H, x, 2048, wq + h0 * 256, wq + h1 * 256, 8192, lds0, hw, lane);
#pragma unroll
      for (int s = 0; s < 4; ++s) h_issue<W_NT, X_NT>(H, KROT(s), s);
      vbar<18>();
#pragma unroll 1
      for (int s = 0; s < 28; ++s) { vbar<12>();
        if (((s + 4) & (RB - 1)) < NTN) h_issue<W_NT, X_NT>(H, KROT(s + 4), s & 3); else h_issue<W_NT2, X_NT2>(H, KROT(s + 4), s & 3); }
      vbar<12>(); h_kv(A, 0);
      vbar<10>(); h_kv(A, 1);
      vbar<8>();
      bar(); h_kv(A, 2);
      bar();
      { u64 seed; asm volatile("s_load_dwordx2 %0, %1, 0x0\n\ts_waitcnt lgkmcnt(0)" : "=s"(seed) : "s"(fl) : "memory"); ep = mix64(seed); }
      bar();
      vbar<8>(); h_kv(A, 3); h_kv(A, 4);
#pragma unroll 1
      for (int n = 0; n < 13; ++n) {
        vbar<8>();
        if (n == 8) h_ostore(L + OS0OFF + hw * XST, Ob + (h0 * 4 + hw) * XST, lane);
        if (PF && n >= 2 && n < 10) {
          const int r0 = ((n - 2) * 4 + (b >> 1)) * 32 + hw * 8;
          const char* src = wo + (size_t)(r0 + (r0 >> 9) * 1536) * 2048;
#pragma unroll
          for (int i = 0; i < 4; ++i) glds<PF_NT>(src + i * 4096, (lane >> 5) * 2048 + (lane & 31) * 16, lds0 + PFOFF + hw * 1024);
        }
        h_kv(A, 2 * n + 5); h_kv(A, 2 * n + 6);
        if (n == 9 && tid == 256) __hip_atomic_store(oflag + c, ep, RLX_AGENT);
      }
      vbar<8>(); h_kv(A, 31);
      vbar<4>();
      wait_flags4(oflag, ep, lane);
      bar();
    } else {
      const int r4 = ((b >> 1) + c) & 3;
#define KROT4(s) (((s) & ~3) | (((s) + r4) & 3))
      HG2 H; h_setup128(H, x, wq + h0 * 256, lds0, hw, lane);
#pragma unroll 1
      for (int s = 0; s < 3; ++s) h_issue128<W_NT, X_NT>(H, KROT4(s), s);
      { u64 seed; asm volatile("s_load_dwordx2 %0, %1, 0x0\n\ts_waitcnt lgkmcnt(0)" : "=s"(seed) : "s"(fl) : "memory"); ep = mix64(seed); }
#pragma unroll 1
      for (int ph = 0; ph < 2; ++ph) {
        vbar<16>();
        if (ph) h_ostore(L + hw * XST, Ob + (h0 * 4 + hw) * XST, lane);
        int sl = 0;
#pragma unroll 1
        for (int s = 0; s < 13; ++s) {
          vbar<8>();
          if (((s + 3) & 3) == 0) h_issue128<W_NT, X_NT>(H, KROT4(s + 3), sl); else h_issue128<W_NT2, X_NT2>(H, KROT4(s + 3), sl);
          sl = sl == 2 ? 0 : sl + 1;
          if (ph && s == 2 && tid == 256) __hip_atomic_store(oflag + c, ep, RLX_AGENT);
        }
        vbar<8>(); h_kv(A, 0);
        vbar<4>(); h_kv(A, 1);
        bar();
        bar(); h_kv(A, 2);
        bar();
        vbar<8>(); h_kv(A, 3); h_kv(A, 4);
#pragma unroll 1
        for (int n = 0; n < 5; ++n) { vbar<8>();
          if (PF && ph == 0 && n < 4) {
            const char* srcw = wq + h1 * 256 + (size_t)((n * 4 + (b >> 1)) * 64 + hw * 16) * 8192;
#pragma unroll
            for (int i = 0; i < 4; ++i) glds<PF_NT>(srcw + (size_t)i * 4 * 8192, (lane >> 4) * 8192 + (lane & 15) * 16, lds0 + PFOFF + hw * 1024);
            const char* srcx = x + (size_t)(28 * c + 7 * hw) * 2048 + n * 512;
#pragma unroll
            for (int i = 0; i < 4; ++i) glds<0>(srcx + (i < 3 ? i : 2) * 4096 + (i == 3 ? 2048 : 0), (lane >> 5) * 2048 + (lane & 31) * 16, lds0 + PFOFF + hw * 1024);
          }
          h_kv(A, 2 * n + 5); h_kv(A, 2 * n + 6); }
        vbar<8>(); h_kv(A, 15);
        vbar<4>();
        if (ph == 0) {
          H.w += 16 * 256;
          vbar<0>();
#pragma unroll 1
          for (int s = 0; s < 3; ++s) h_issue128<W_NT, X_NT>(H, KROT4(s), s);
          A.k0 += 16 * 131072; A.v0 += 16 * 131072;
        } else {
          wait_flags4(oflag, ep, lane);
          bar();
        }
      }
    }
  }
  if (wid < 4) {
    f32x4 acc[7][4];
    c_gemm(acc, L, wid, lane, true);
    STAMP(5);
    lbar();
    if (wid != j) {
#pragma unroll
      for (int mt = 0; mt < 7; ++mt)
#pragma unroll
        for (int e = 0; e < 4; e += 2) { const u32x4 pk = {cvtpk(acc[mt][e][0], acc[mt][e][1]), cvtpk(acc[mt][e][2], acc[mt][e][3]), cvtpk(acc[mt][e + 1][0], acc[mt][e + 1][1]), cvtpk(acc[mt][e + 1][2], acc[mt][e + 1][3])};
          __builtin_amdgcn_raw_buffer_store_b128(pk, prs, lane * 16 + ((wid * 4 + j) * 28 + mt * 2 + (e >> 1)) * 1024, 0, 16); }
    } else {
#pragma unroll
      for (int mt = 0; mt < 7; ++mt)
#pragma unroll
        for (int e = 0; e < 4; ++e) *(LAS f32x4*)(L + QXOFF + (mt * 4 + e) * 1024 + lane * 16) = acc[mt][e];
    }
    vlbar();
    STAMP(6);
    bar();
    STAMP(7);
  } else {
    int hz = hw, lz = lane; asm volatile("" : "+s"(hz)); asm volatile("" : "+v"(lz));
    HG H; h_setup(H, Ob + j * 16 * XST, 0, wo, wo + 256, 2048, lds0, hz, lz);
    u64 fv = 0;
#pragma unroll 1
    for (int s = 0; s < 4; ++s) h_issue<W_NT, 2>(H, KROT(s), s);
    vbar<18>();
    h_ostore(L + (grpB ? 0 : OS1OFF) + hz * XST, Ob + (h1 * 4 + hz) * XST, lz);
#pragma unroll 1
    for (int s = 0; s < 28; ++s) {
      vbar<12>();
      if (s == 9) asm volatile("global_load_dwordx2 %0, %1, %2 sc1" : "=v"(fv) : "v"((lz & 3) * 8 + 32), "s"(oflag) : "memory");
      if (s == 12) { asm volatile("" : "+v"(fv)); if (!__all(fv == ep)) wait_flags4(oflag + 4, ep, lz); }
      const int n = s + 4, kb = (n >> 4) * 48 + KROT(n);
      if ((n & (RB - 1)) < NTN) h_issue<W_NT, 2>(H, kb, s & 3); else h_issue<W_NT2, 2>(H, kb, s & 3);
      if (s == 3 && tid == 256) __hip_atomic_store(oflag + 4 + c, ep, RLX_AGENT);
    }
    vbar<12>(); vbar<6>(); vbar<0>(); bar();
    bar();
    bar();
    if (hw == 0) { if (lane == 0) __hip_atomic_store(pflag + j, ep, RLX_AGENT); wait_flags4(pflag, ep, lane); }
    vbar<0>();
    if (bid == 0 && hw == 3) {
#pragma unroll 1
      for (unsigned spins = 0; spins < (1u << 17); ++spins) {
        bool ok = true;
#pragma unroll
        for (int k = 0; k < 4; ++k) ok &= __hip_atomic_load(fl + 512 + lane * 8 + 4 + k, RLX_AGENT) == ep;
        if (__all(ok)) break;
        __builtin_amdgcn_s_sleep(2);
      }
      if (lane == 0) __hip_atomic_store(fl, ep, RLX_AGENT);
    }
  }
  if (wid < 7) {
    f32x4 in[3][4];
#pragma unroll
    for (int k = 0; k < 3; ++k) { const int src = (j + 1 + k) & 3;
#pragma unroll
      for (int e = 0; e < 4; e += 2) { const u32x4 pk = __builtin_amdgcn_raw_buffer_load_b128(prs, lane * 16, ((j * 4 + src) * 28 + wid * 2 + (e >> 1)) * 1024, 16);
        in[k][e] = (f32x4){__uint_as_float(pk[0] << 16), __uint_as_float(pk[0] & 0xffff0000u), __uint_as_float(pk[1] << 16), __uint_as_float(pk[1] & 0xffff0000u)};
        in[k][e + 1] = (f32x4){__uint_as_float(pk[2] << 16), __uint_as_float(pk[2] & 0xffff0000u), __uint_as_float(pk[3] << 16), __uint_as_float(pk[3] & 0xffff0000u)}; }
    }
    const int q = lane & 15, g = lane >> 4;
    f32x4 sum[4];
#pragma unroll
    for (int e = 0; e < 4; ++e) sum[e] = *(LAS const f32x4*)(L + QXOFF + (wid * 4 + e) * 1024 + lane * 16) + in[0][e] + (in[1][e] + in[2][e]);
#pragma unroll
    for (int pr = 0; pr < 2; ++pr)
      *(GAS u32x4*)(out + ((size_t)(b * 112 + 16 * wid + q) * 2048 + br * 1024 + c * 256 + j * 64 + pr * 32 + g * 8) * 2) = pack32(sum[2 * pr], sum[2 * pr + 1]);
  }
  STAMP(8); CSTAMP(1);
}

extern "C" void kernel_launch(void* const* d_in, const int* in_sizes, int n_in, void* d_out, int out_size, void* d_ws, size_t ws_size, hipStream_t stream) {
  (void)in_sizes; (void)n_in; (void)out_size; (void)ws_size;
  (void)hipFuncSetAttribute((const void*)fused, hipFuncAttributeMaxDynamicSharedMemorySize, LDS_BYTES);
  fused<<<256, 512, LDS_BYTES, stream>>>((const char*)d_in[0], (const char*)d_in[1], (const char*)d_in[2], (const char*)d_in[3], (const char*)d_in[4],
                                         (const char*)d_in[5], (const char*)d_in[6], (const char*)d_in[7], (const char*)d_in[8], (const char*)d_in[9], (char*)d_ws, (char*)d_out);
}
```

```cpp
#include <hip/hip_runtime.h>
#include <hip/hip_bf16.h>
#include <stdint.h>

typedef __attribute__((ext_vector_type(8))) short bf16x8;
typedef __attribute__((ext_vector_type(4))) short s16x4;
typedef __attribute__((ext_vector_type(4))) float f32x4;
typedef __attribute__((ext_vector_type(2))) float f32x2;
typedef __attribute__((ext_vector_type(16))) float f32x16;
typedef __attribute__((ext_vector_type(4))) unsigned u32x4;
typedef __attribute__((ext_vector_type(2))) __bf16 bf16x2_t;
typedef short v4i16_t __attribute__((ext_vector_type(4)));
typedef unsigned long long u64;
#define LAS __attribute__((address_space(3)))
#define GAS __attribute__((address_space(1)))
#define DI __device__ __forceinline__
#define MFMA16(a, b, c) __builtin_amdgcn_mfma_f32_16x16x32_bf16((a), (b), (c), 0, 0, 0)
#define MFMA32(a, b, c) __builtin_amdgcn_mfma_f32_32x32x16_bf16((a), (b), (c), 0, 0, 0)
#define SBAR() __builtin_amdgcn_sched_barrier(0)
#define RLX_AGENT __ATOMIC_RELAXED, __HIP_MEMORY_SCOPE_AGENT

#define W_NT 0
#define W_NT2 0
#define RB 8
#define PBF16 1
#define STAGGER 1
#define PF 0
#define PF_NT 0
#define X_NT 0
#define X_NT2 0
#define KV_NT 1
#define WARMUP 3
#define NTN 2
#define ROT 1
#define THR 8.0f

constexpr int LDS_BYTES = 163840;
constexpr int SLOT = 16384;
constexpr int WOFF = 65536, WST = 16384;
constexpr int XOFF = 131072, XST = 7168;
constexpr int QXOFF = 65536;
constexpr int Q1OFF = 131072;
constexpr int Q0OFF = 98304;
constexpr int OS0OFF = Q0OFF, OS1OFF = 2 * SLOT;
constexpr int PFOFF = 159744;
constexpr int NSL = 6;
constexpr size_t WS_O = 0;
constexpr size_t WS_PART = 16u << 20;
constexpr size_t PART_TILE = 16 * 28 * 1024;
constexpr size_t WS_FLAG = 64u << 20;

template <int NT>
DI void glds(const void* sbase, unsigned voff, unsigned ldsdst) {
  if (NT == 1) asm volatile("s_mov_b32 m0, %2\n\ts_nop 3\n\tglobal_load_lds_dwordx4 %0, %1 nt" ::"v"(voff), "s"(sbase), "s"(ldsdst) : "memory");
  else if (NT == 2) asm volatile("s_mov_b32 m0, %2\n\ts_nop 3\n\tglobal_load_lds_dwordx4 %0, %1 sc1" ::"v"(voff), "s"(sbase), "s"(ldsdst) : "memory");
  else asm volatile("s_mov_b32 m0, %2\n\ts_nop 3\n\tglobal_load_lds_dwordx4 %0, %1" ::"v"(voff), "s"(sbase), "s"(ldsdst) : "memory");
}
DI void touch(const char* addr, unsigned ldsdst) {
  asm volatile("s_mov_b32 m0, %1\n\ts_nop 3\n\tglobal_load_lds_dword %0, off" ::"v"(addr), "s"(ldsdst) : "memory");
}
template <int N> DI void vbar() { asm volatile("s_waitcnt vmcnt(%0)\n\ts_barrier" ::"i"(N) : "memory"); }
template <int N> DI void wait_vm() { asm volatile("s_waitcnt vmcnt(%0)" ::"i"(N) : "memory"); }
DI void lbar() { asm volatile("s_waitcnt lgkmcnt(0)\n\ts_barrier" ::: "memory"); }
DI void bar() { asm volatile("s_barrier" ::: "memory"); }
DI unsigned cvtpk(float lo, float hi) { const f32x2 v = {lo, hi}; return __builtin_bit_cast(unsigned, __builtin_convertvector(v, bf16x2_t)); }
#define STAMP(i) do {} while (0)
#define CSTAMP(i) do {} while (0)
#define HSTAMP(i) do {} while (0)
DI s16x4 trread(LAS const char* p) { return __builtin_bit_cast(s16x4, __builtin_amdgcn_ds_read_tr16_b64_v4i16((LAS v4i16_t*)p)); }
DI bf16x8 ldsr(LAS const char* p) { return *(LAS const bf16x8*)p; }
DI bf16x8 cat8(s16x4 a, s16x4 b) { return __builtin_shufflevector(a, b, 0, 1, 2, 3, 4, 5, 6, 7); }
DI int fsw(int row) { return (row & 3) | ((((row >> 2) ^ (row >> 3)) & 1) << 2); }
DI u64 mix64(u64 z) { z += 0x9E3779B97F4A7C15ull; z = (z ^ (z >> 30)) * 0xBF58476D1CE4E5B9ull; z = (z ^ (z >> 27)) * 0x94D049BB133111EBull; return z ^ (z >> 31); }

DI u32x4 pack32(f32x4 a, f32x4 b) {
  auto r0 = __builtin_amdgcn_permlane32_swap(cvtpk(a[0], a[1]), cvtpk(b[0], b[1]), false, false);
  auto r1 = __builtin_amdgcn_permlane32_swap(cvtpk(a[2], a[3]), cvtpk(b[2], b[3]), false, false);
  auto s0 = __builtin_amdgcn_permlane16_swap(r0[0], r0[1], false, false);
  auto s1 = __builtin_amdgcn_permlane16_swap(r1[0], r1[1], false, false);
  u32x4 o = {s0[0], s1[0], s0[1], s1[1]};
  return o;
}
DI float rowmax4(float mx) {
  auto r = __builtin_amdgcn_permlane32_swap(__float_as_uint(mx), __float_as_uint(mx), false, false);
  mx = __builtin_fmaxf(__uint_as_float(r[0]), __uint_as_float(r[1]));
  auto r2 = __builtin_amdgcn_permlane16_swap(__float_as_uint(mx), __float_as_uint(mx), false, false);
  return __builtin_fmaxf(__uint_as_float(r2[0]), __uint_as_float(r2[1]));
}
DI float max4(float m, const f32x4& c) { return __builtin_fmaxf(__builtin_fmaxf(__builtin_fmaxf(m, c[0]), c[1]), __builtin_fmaxf(c[2], c[3])); }

template <bool VM>
DI void c_stage(f32x4 (&acc)[7][4], bf16x8 (&X)[7], const bf16x8 (&W)[4], bf16x8 (&Wn)[4], LAS const char* xr, LAS const char* const (&wr)[8], int cs, int ns) {
  s16x4 lo[4];
#pragma unroll
  for (int gp = 0; gp < 28; ++gp) {
    const int mt = gp >> 2, e = gp & 3;
    acc[mt][e] = MFMA16(W[e], X[mt], acc[mt][e]);
    SBAR();
    if (gp == 0) X[6] = ldsr(xr + cs * XST + 6 * 1024);
    if (gp == 5) { if (VM) asm volatile("s_waitcnt vmcnt(0) lgkmcnt(0)\n\ts_barrier" ::: "memory"); else lbar(); }
    if (gp >= 6 && gp < 22 && (gp & 1) == 0) { const int k = (gp - 6) >> 1;
      if ((k & 1) == 0) lo[k >> 1] = trread(wr[k >> 1] + ns * WST); else Wn[k >> 1] = cat8(lo[k >> 1], trread(wr[4 + (k >> 1)] + ns * WST + 1024)); }
    if (gp >= 7 && (gp & 3) == 3) X[mt - 1] = ldsr(xr + ns * XST + (mt - 1) * 1024);
    SBAR();
  }
}
DI void c_gemm(f32x4 (&acc)[7][4], LAS char* L, int wid, int lane, bool xlin) {
  asm volatile("" : "+v"(lane));
  const int q = lane & 15, g = lane >> 4;
  LAS const char* xr = L + XOFF + (xlin ? lane * 16 : q * 64 + ((g ^ ((-(q >> 2)) & 3)) << 4));
  LAS const char* wr[8];
  { const int row = 8 * g + (q >> 2), p = q & 3, f = fsw(row), nt0 = 4 * (wid & 1);
    LAS const char* wb = L + WOFF + (wid >> 1) * 8192 + row * 256 + p * 8;
#pragma unroll
    for (int e = 0; e < 4; ++e) { wr[e] = wb + (((nt0 + e) ^ f) << 5); wr[4 + e] = wb + (((nt0 + e) ^ f ^ 4) << 5); } }
#pragma unroll
  for (int mt = 0; mt < 7; ++mt)
#pragma unroll
    for (int e = 0; e < 4; ++e) acc[mt][e] = (f32x4){0.f, 0.f, 0.f, 0.f};
  bf16x8 X[7], Wa[4], Wb[4];
  lbar();
#pragma unroll
  for (int e = 0; e < 4; ++e) Wa[e] = cat8(trread(wr[e]), trread(wr[4 + e] + 1024));
#pragma unroll
  for (int mt = 0; mt < 6; ++mt) X[mt] = ldsr(xr + mt * 1024);
#pragma unroll 1
  for (int s = 0; s < 32; s += 4) {
    c_stage<false>(acc, X, Wa, Wb, xr, wr, 0, 1);
    c_stage<false>(acc, X, Wb, Wa, xr, wr, 1, 2);
    c_stage<false>(acc, X, Wa, Wb, xr, wr, 2, 3);
    c_stage<true>(acc, X, Wb, Wa, xr, wr, 3, 0);
  }
}

constexpr int DST = 30720;
DI void c_stage128(f32x4 (&acc)[7][2], bf16x8 (&X)[14], const bf16x8 (&W)[4], bf16x8 (&Wn)[4], LAS const char*& xr, LAS const char* (&wr)[4], int dn) {
  s16x4 lo[4];
#pragma unroll
  for (int gp = 0; gp < 28; ++gp) {
    const int i = gp >> 1, e = gp & 1, kh = i / 7, mt = i % 7;
    acc[mt][e] = MFMA16(W[2 * kh + e], X[i], acc[mt][e]);
    SBAR();
    if (gp < 3) X[11 + gp] = ldsr(xr + (11 + gp) * 1024);
    if (gp == 3) { xr += dn; wr[0] += dn; wr[1] += dn; }
    if (gp == 4) { wr[2] += dn; wr[3] += dn; }
    if (gp == 6) lbar();
    if (gp >= 7 && (gp & 1) == 1) { const int z = (gp - 7) >> 1; X[z] = ldsr(xr + z * 1024); }
    if (gp >= 8 && gp < 24 && (gp & 1) == 0) { const int k = (gp - 8) >> 1, f = k >> 1;
      if ((k & 1) == 0) lo[f] = trread(wr[f & 1] + (f >> 1) * 8192); else Wn[f] = cat8(lo[f], trread(wr[2 + (f & 1)] + (f >> 1) * 8192 + 1024)); }
    SBAR();
  }
}
DI void c_gemm128(f32x4 (&acc)[7][2], LAS char* L, int wid, int lane) {
  asm volatile("" : "+v"(lane));
  const int q = lane & 15, g = lane >> 4;
  LAS const char* xr = L + WOFF + 16384 + q * 64 + ((g ^ ((-(q >> 2)) & 3)) << 4);
  LAS const char* wr[4];
  { const int row = 8 * g + (q >> 2), p = q & 3, f = fsw(row), nt0 = 2 * wid;
    LAS const char* wb = L + WOFF + row * 256 + p * 8;
#pragma unroll
    for (int e = 0; e < 2; ++e) { wr[e] = wb + (((nt0 + e) ^ f) << 5); wr[2 + e] = wb + (((nt0 + e) ^ f ^ 4) << 5); } }
#pragma unroll
  for (int mt = 0; mt < 7; ++mt) { acc[mt][0] = (f32x4){0.f, 0.f, 0.f, 0.f}; acc[mt][1] = (f32x4){0.f, 0.f, 0.f, 0.f}; }
  bf16x8 X[14], Wa[4], Wb[4];
  lbar();
#pragma unroll
  for (int f = 0; f < 4; ++f) Wa[f] = cat8(trread(wr[f & 1] + (f >> 1) * 8192), trread(wr[2 + (f & 1)] + (f >> 1) * 8192 + 1024));
#pragma unroll
  for (int i = 0; i < 11; ++i) X[i] = ldsr(xr + i * 1024);
  int sl = 0;
#pragma unroll 1
  for (int s = 0; s < 16; s += 2) {
    c_stage128(acc, X, Wa, Wb, xr, wr, sl == 2 ? -2 * DST : DST); sl = sl == 2 ? 0 : sl + 1;
    c_stage128(acc, X, Wb, Wa, xr, wr, sl == 2 ? -2 * DST : DST); sl = sl == 2 ? 0 : sl + 1;
  }
}

#define PIN(x) asm volatile("" : "+v"(x))
DI float mx3(float a, float b, float c) { return __builtin_elementwise_maximum(__builtin_elementwise_maximum(a, b), c); }
DI void vlbar() { asm volatile("s_waitcnt vmcnt(0) lgkmcnt(0)\n\ts_barrier" ::: "memory"); }

struct HA { const char* k0; const char* v0; unsigned kv[4], vv[4]; unsigned d; };
DI void h_kv(const HA& A, int T) {
  const int n = T >> 1; const unsigned so = (unsigned)(n >> 3) * (16u * 131072u) + (n & 7) * SLOT;
  const unsigned d = A.d + (T % NSL) * SLOT;
  if (T & 1) {
#pragma unroll
    for (int i = 0; i < 4; ++i) glds<KV_NT>(A.v0 + so, A.vv[i], d + i * 1024);
  } else {
#pragma unroll
    for (int i = 0; i < 4; ++i) glds<KV_NT>(A.k0 + so, A.kv[i], d + i * 1024);
  }
}
DI void h_kv_setup(HA& A, const char* k0, const char* v0, int row0  , unsigned lds0, int lane) {
  A.k0 = k0 + row0 * 256; A.v0 = v0 + row0 * 256; A.d = lds0 + row0 * 256;
#pragma unroll
  for (int i = 0; i < 4; ++i) { const int row = 4 * i + (lane >> 4); A.kv[i] = row * 256 + (((lane & 15) ^ row) << 4); A.vv[i] = row * 256 + (((lane & 15) ^ ((row & 3) << 2)) << 4); }
}
DI void h_ostore(LAS const char* src  , char* dst, int lane) {
  const __amdgpu_buffer_rsrc_t ors = __builtin_amdgcn_make_buffer_rsrc(dst, 0, XST, 0x00020000);
  u32x4 v[7];
#pragma unroll
  for (int i = 0; i < 7; ++i) v[i] = *(LAS const u32x4*)(src + i * 1024 + lane * 16);
#pragma unroll
  for (int i = 0; i < 7; ++i) __builtin_amdgcn_raw_buffer_store_b128(v[i], ors, i * 1024 + lane * 16, 0, 16);
}
DI void wait_flags4(GAS u64* f, u64 e, int lane) {
#pragma unroll 1
  for (unsigned spins = 0; spins < (1u << 17); ++spins) {
    const u64 v = __hip_atomic_load(f + (lane & 3), RLX_AGENT);
    if (__all(v == e)) break;
    __builtin_amdgcn_s_sleep(1);
  }
}
DI float xhalf_max(float m) { auto r = __builtin_amdgcn_permlane32_swap(__float_as_uint(m), __float_as_uint(m), false, false); return __builtin_fmaxf(__uint_as_float(r[0]), __uint_as_float(r[1])); }
DI float xhalf_sum(float m) { auto r = __builtin_amdgcn_permlane32_swap(__float_as_uint(m), __float_as_uint(m), false, false); return __uint_as_float(r[0]) + __uint_as_float(r[1]); }
struct AttnS { f32x16 O[4], nm, Ls; bf16x8 Qf[8], kf[8], ones; unsigned w0, w1; float mx; bool big; };
#define LGKM(n) __builtin_amdgcn_s_waitcnt(0xC07F | ((n) << 8))
#define EXP2(x) __builtin_amdgcn_exp2f(x)

template <int GK>
DI void a_grp(AttnS& A, f32x16& So, f32x16& Sn, LAS const char* (&ka)[8], LAS const char* (&va)[4], int dk) {
  constexpr int NT[18] = {0, 1, 2, 1, 2, 1, 2, 1, 2, 1, 2, 1, 0, 0, 0, 0, 0, 0};
  constexpr int NB[18] = {0, 0, 0, 0, 0, 0, 0, 0, 0, 0, 0, 1, 1, 1, 2, 1, 1, 1};
  constexpr int NM[18] = {0, 0, 0, 0, 0, 0, 0, 0, 0, 0, 0, 1, 3, 3, 1, 0, 0, 0};
  u32x4 pw[2]; s16x4 vt[16]; float e0 = 0.f, e1 = 0.f, e2 = 0.f;
  pw[0][0] = A.w0; pw[0][1] = A.w1;
  int it = 0, ib = 0, im = 0;
#pragma unroll
  for (int sl = 0; sl < 18; ++sl) {
    if (sl == 0) LGKM(4);
    if (sl == 4 && GK == 1) LGKM(4);
    if (sl == 9) LGKM(4);
    if (sl == 14) LGKM(3);
    if (sl < 8) Sn = MFMA32(A.kf[sl], A.Qf[sl], sl == 0 ? A.nm : Sn);
    else { const int u = (sl - 8) / 5, r = (sl - 8) % 5; const bf16x8 pf = __builtin_bit_cast(bf16x8, pw[u]);
      if (r == 0) A.Ls = MFMA32(A.ones, pf, A.Ls); else { const int z = 4 * u + r - 1; A.O[r - 1] = MFMA32(cat8(vt[2 * z], vt[2 * z + 1]), pf, A.O[r - 1]); } }
    SBAR();
    if (sl >= 1 && sl <= 11 && (sl & 1)) { const int p = 2 + (sl >> 1); unsigned w = cvtpk(So[2 * p], So[2 * p + 1]); PIN(w); pw[p >> 2][p & 3] = w; }
    if (sl == 0) { float x = EXP2(So[4]); PIN(x); So[4] = x; }
    if (sl <= 10) { float x = EXP2(So[5 + sl]); PIN(x); So[5 + sl] = x; }
    if (GK == 0 && sl == 0) { PIN(Sn); LGKM(0); bar(); }
#pragma unroll
    for (int k = 0; k < NT[sl]; ++k) { const int z = it >> 1;
      vt[it] = trread(va[z & 3] + GK * 8192 + (z >> 2) * 4096 + (it & 1) * 2048); ++it; }
    if (GK == 0 && sl >= 1 && sl <= 8) { ka[sl - 1] += dk; PIN(ka[sl - 1]); }
#pragma unroll
    for (int k = 0; k < NB[sl]; ++k) { A.kf[ib] = ldsr(ka[ib] + GK * 8192); ++ib; }
#pragma unroll
    for (int k = 0; k < NM[sl]; ++k) { float x = mx3(im == 0 ? Sn[0] : A.mx, Sn[2 * im], Sn[2 * im + 1]); PIN(x); A.mx = x; ++im; }
    if (sl == 14) A.big = __any(A.mx > THR);
    if (GK == 1 && sl >= 12 && sl <= 15) { va[sl - 12] += dk; PIN(va[sl - 12]); }
    if (sl == 15) { e0 = EXP2(Sn[0]); e1 = EXP2(Sn[1]); PIN(e0); PIN(e1); }
    if (sl == 16) { unsigned w = cvtpk(e0, e1); PIN(w); A.w0 = w; e2 = EXP2(Sn[2]); PIN(e2); }
    if (sl == 17) { unsigned w = cvtpk(e2, EXP2(Sn[3])); PIN(w); A.w1 = w; }
    SBAR();
  }
}
DI void a_check(AttnS& A, f32x16& Sn, bool first) {
  if (first || A.big) {
    float d = xhalf_max(A.mx);
    if (!first) d = __builtin_fmaxf(d, 0.f);
    const float f = first ? 0.f : EXP2(-d);
    A.nm -= d; Sn -= d;
#pragma unroll
    for (int dt = 0; dt < 4; ++dt) A.O[dt] *= f;
    A.Ls *= f;
    A.w0 = cvtpk(EXP2(Sn[0]), EXP2(Sn[1])); A.w1 = cvtpk(EXP2(Sn[2]), EXP2(Sn[3]));
  }
}
DI void a_loadq(AttnS& A, LAS const char* Qi, int wid, int lane) {
  const int q = lane & 31, h = lane >> 5; int mt = 2 * wid + (q >> 4); mt = mt > 6 ? 6 : mt;
  LAS const char* p = Qi + mt * 4096 + (h * 16 + (q & 15)) * 16;
#pragma unroll
  for (int s = 0; s < 8; ++s) A.Qf[s] = ldsr(p + (s >> 1) * 1024 + (s & 1) * 512);
#pragma unroll
  for (int i = 0; i < 16; ++i) A.nm[i] = 0.f;
}
DI void c_attn(LAS char* L, int wid, int lane, int nper  ) {
  asm volatile("" : "+v"(lane));
  const int r = lane & 31, h = lane >> 5;
  LAS const char* ka[8]; LAS const char* va[4];
#pragma unroll
  for (int s = 0; s < 8; ++s) ka[s] = L + r * 256 + (((h ^ (r & 15)) << 4) ^ (s << 5));
  { const int db = (lane >> 4) & 1, qq = (lane & 15) >> 2, p = lane & 3;
#pragma unroll
    for (int dt = 0; dt < 4; ++dt) va[dt] = L + SLOT + (4 * h + qq) * 256 + (((2 * dt + db) ^ (qq << 1)) << 5) + p * 8; }
  AttnS A; f32x16 SA, SB;
#pragma unroll
  for (int dt = 0; dt < 4; ++dt)
#pragma unroll
    for (int i = 0; i < 16; ++i) A.O[dt][i] = 0.f;
#pragma unroll
  for (int i = 0; i < 16; ++i) A.Ls[i] = 0.f;
  A.ones = (bf16x8){0x3F80, 0x3F80, 0x3F80, 0x3F80, 0x3F80, 0x3F80, 0x3F80, 0x3F80}; PIN(A.ones);
  a_loadq(A, L + Q0OFF, wid, lane);
  bar();
#pragma unroll
  for (int s = 0; s < 8; ++s) SA = MFMA32(ldsr(ka[s]), A.Qf[s], s == 0 ? A.nm : SA);
#pragma unroll
  for (int s = 0; s < 8; ++s) A.kf[s] = ldsr(ka[s] + 8192);
  A.mx = SA[0];
#pragma unroll
  for (int i = 0; i < 16; i += 2) A.mx = mx3(A.mx, SA[i], SA[i + 1]);
  a_check(A, SA, true);
  int ks = 0;
#pragma unroll 1
  for (int n = 0; n < nper; ++n) {
    const int dk = ks == 2 ? -4 * SLOT : 2 * SLOT;
    a_grp<0>(A, SA, SB, ka, va, dk);
    a_check(A, SB, false);
    if (n == 7) a_loadq(A, L + Q1OFF, wid, lane);
    a_grp<1>(A, SB, SA, ka, va, dk);
    if ((n & 7) == 7) {
      const float inv = __builtin_amdgcn_rcpf(A.Ls[0]);
      LAS char* os = L + (nper == 8 ? 0 : (n == 7 ? OS0OFF : OS1OFF)) + 2 * wid * 1024 + ((r >> 4) * 64 + h * 16 + (r & 15)) * 16;
      if (wid < 3 || r < 16) {
#pragma unroll
        for (int dt = 0; dt < 4; ++dt)
#pragma unroll
          for (int g = 0; g < 4; g += 2) {
            auto x0 = __builtin_amdgcn_permlane32_swap(cvtpk(A.O[dt][4 * g] * inv, A.O[dt][4 * g + 1] * inv), cvtpk(A.O[dt][4 * g + 4] * inv, A.O[dt][4 * g + 5] * inv), false, false);
            auto x1 = __builtin_amdgcn_permlane32_swap(cvtpk(A.O[dt][4 * g + 2] * inv, A.O[dt][4 * g + 3] * inv), cvtpk(A.O[dt][4 * g + 6] * inv, A.O[dt][4 * g + 7] * inv), false, false);
            const u32x4 o = {x0[0], x1[0], x0[1], x1[1]};
            *(LAS u32x4*)(os + dt * XST + g * 256) = o;
          }
      }
    }
    if (n < nper - 1) a_check(A, SA, n == 7);
    ks = ks == 2 ? 0 : ks + 1;
  }
}

struct HG { const char* w; const char* x; unsigned ldw, xstep; unsigned wv[4], xv[2]; unsigned wd, xd, xd1; };
DI void h_setup(HG& H, const char* x, unsigned ldx, const char* w0, const char* w1, unsigned ldw, unsigned lds0, int hw, int lane) {
  H.w = ((hw >> 1) ? w1 : w0) + (size_t)(16 * (hw & 1)) * ldw; H.ldw = ldw;
#pragma unroll
  for (int i = 0; i < 4; ++i) { const int row = 4 * i + (lane >> 4); H.wv[i] = row * ldw + (((lane & 15) ^ (fsw(row) << 1)) << 4); }
  if (ldx) {
    H.x = x + (size_t)(32 * hw) * ldx; H.xstep = 64;
    const unsigned xo = (lane >> 2) * ldx + (((lane & 3) ^ ((-(lane >> 4)) & 3)) << 4);
    H.xv[0] = xo; H.xv[1] = xo + (hw < 3 ? 16 * ldx : 0);
  } else {
    H.x = x + hw * 2048; H.xstep = XST; H.xv[0] = lane * 16; H.xv[1] = lane * 16 + (hw < 3 ? 1024 : 0);
  }
  H.wd = lds0 + WOFF + hw * 4096; H.xd = lds0 + XOFF + hw * 2048; H.xd1 = hw < 3 ? 1024 : 0;
}
template <int NT, int XP>
DI void h_issue(const HG& H, int kb  , int slot) {
  const char* w = H.w + (size_t)kb * 32 * H.ldw; const char* x = H.x + kb * H.xstep;
#pragma unroll
  for (int i = 0; i < 4; ++i) glds<NT>(w, H.wv[i], H.wd + slot * WST + i * 1024);
  glds<XP>(x, H.xv[0], H.xd + slot * XST); glds<XP>(x, H.xv[1], H.xd + slot * XST + H.xd1);
}
struct HG2 { const char* w; const char* x; unsigned wv[4], xv[4]; unsigned wd, xd, xd3; };
DI void h_setup128(HG2& H, const char* x, const char* w, unsigned lds0, int hw, int lane) {
  H.w = w + (size_t)(16 * hw) * 8192;
#pragma unroll
  for (int i = 0; i < 4; ++i) { const int row = 4 * i + (lane >> 4); H.wv[i] = row * 8192 + (((lane & 15) ^ (fsw(row) << 1)) << 4); }
  H.x = x + (size_t)(64 * (hw & 1)) * 2048 + (hw >> 1) * 64;
  const unsigned xo = (lane >> 2) * 2048 + (((lane & 3) ^ ((-(lane >> 4)) & 3)) << 4);
#pragma unroll
  for (int i = 0; i < 4; ++i) H.xv[i] = xo + ((i == 3 && (hw & 1)) ? 2 : i) * 16 * 2048;
  H.wd = lds0 + WOFF + hw * 4096; H.xd = lds0 + WOFF + 16384 + (hw >> 1) * XST + (hw & 1) * 4096; H.xd3 = (hw & 1) ? 2048 : 3072;
}
template <int NT, int XP>
DI void h_issue128(const HG2& H, int kd  , int slot) {
  const char* w = H.w + (size_t)kd * 64 * 8192; const char* x = H.x + kd * 128;
#pragma unroll
  for (int i = 0; i < 4; ++i) glds<NT>(w, H.wv[i], H.wd + slot * DST + i * 1024);
#pragma unroll
  for (int i = 0; i < 4; ++i) glds<XP>(x, H.xv[i], H.xd + slot * DST + (i == 3 ? H.xd3 : i * 1024));
}

__global__ __launch_bounds__(512, 2) void fused(const char* xA, const char* kA, const char* vA, const char* wqA, const char* woA,
                                                const char* xB, const char* kB, const char* vB, const char* wqB, const char* woB, char* ws, char* out) {
  const char* pc0; asm volatile("s_getpc_b64 %0" : "=s"(pc0));
  extern __shared__ __attribute__((aligned(16))) char lds[];
  LAS char* L = (LAS char*)lds; const unsigned lds0 = (unsigned)(uintptr_t)L;
  const int tid = threadIdx.x, lane = tid & 63; const int wid = __builtin_amdgcn_readfirstlane(tid >> 6);
  const int bid = blockIdx.x, xcd = bid & 7, idx = bid >> 3, br = xcd >> 2, j = xcd & 3, b = idx >> 2, c = idx & 3, h0 = 4 * j + c, h1 = h0 + 16;
  char* Ob = ws + WS_O + (size_t)(br * 8 + b) * (128 * XST);
  const int tile = (br * 8 + b) * 4 + c;
  const __amdgpu_buffer_rsrc_t prs = __builtin_amdgcn_make_buffer_rsrc(ws + WS_PART + (size_t)tile * PART_TILE, 0, (int)PART_TILE, 0x00020000);
  STAMP(0); CSTAMP(0);
  const int hw = wid & 3;
  const char* kc = (br ? kB : kA) + (size_t)(b * 32 + h0) * 131072; const char* vc = (br ? vB : vA) + (size_t)(b * 32 + h0) * 131072;
  GAS u64* fl = (GAS u64*)(ws + WS_FLAG);
  GAS u64* oflag = fl + 512 + ((br * 8 + b) * 4 + j) * 8; GAS u64* pflag = fl + 2048 + tile * 8;
  const int r8 = RB == 16 ? (2 * b + 4 * c) & 15 : RB == 8 ? (b + 2 * c) & 7 : (b + c) & (RB - 1);
#define KROT(s) (((s) & ~(RB - 1)) | (((s) + r8) & (RB - 1)))
  u64 ep = 0;
  const char* wo = (br ? woB : woA) + (size_t)(512 * j) * 2048 + c * 512;
  const bool grpB = STAGGER && (b & 1);
  const float cs = 0.08838834764831845f * 1.4426950408889634f;
  if (wid < 4) {
    if ((WARMUP & 1) && wid == 0) {
      const char* a;
      if (lane < 16) a = ((lane & 1) ? vc : kc) + (size_t)((lane >> 1) & 1) * (16u * 131072u) + (lane >> 2) * 32768;
      else if (lane < 48) { const int r = (lane - 16) * 32; a = wo + (size_t)(r + (r >> 9) * 1536) * 2048; }
      else if (lane < 63) a = ws + WS_PART + (size_t)tile * PART_TILE + (lane - 48) * 28672 + 20480;
      else a = out + (size_t)(b * 112) * 4096;
      touch(a, lds0 + PFOFF);
    }
    if ((WARMUP & 2) && wid >= 1) {
#pragma unroll
      for (int k = 0; k < 2; ++k) touch(pc0 + (((wid - 1) * 2 + k) * 64 + lane) * 64, lds0 + PFOFF + 256 * wid);
    }
#pragma unroll 1
    for (int ph = 0; ph < (grpB ? 2 : 1); ++ph) {
      int lz = lane; asm volatile("" : "+v"(lz));
      if (grpB) {
        f32x4 acc[7][2];
        c_gemm128(acc, L, wid, lane);
        lbar();
#pragma unroll
        for (int mt = 0; mt < 7; ++mt) *(LAS u32x4*)(L + Q0OFF + (mt * 4 + wid) * 1024 + lz * 16) = pack32(acc[mt][0] * cs, acc[mt][1] * cs);
      } else {
        f32x4 acc[7][4];
        c_gemm(acc, L, wid, lane, false);
        lbar();
        LAS char* qi = L + ((wid >> 1) ? Q1OFF : Q0OFF) + 2 * (wid & 1) * 1024 + lz * 16;
#pragma unroll
        for (int mt = 0; mt < 7; ++mt)
#pragma unroll
          for (int e = 0; e < 2; ++e) *(LAS u32x4*)(qi + (mt * 4 + e) * 1024) = pack32(acc[mt][2 * e] * cs, acc[mt][2 * e + 1] * cs);
      }
      lbar();
      STAMP(1 + ph);
      c_attn(L, wid, lane, grpB ? 8 : 16);
      if (ph == 0) STAMP(3);
    }
    STAMP(4);
  } else {
    const char* x = (br ? xB : xA) + (size_t)b * 112 * 2048; const char* wq = br ? wqB : wqA;
    HA A; h_kv_setup(A, kc, vc, 16 * hw, lds0, lane);
    if (!grpB) {
      HG H; h_setup(H, x, 2048, wq + h0 * 256, wq + h1 * 256, 8192, lds0, hw, lane);
      h_issue<W_NT, X_NT>(H, KROT(0), 0); h_issue<W_NT, X_NT>(H, KROT(1), 1);
      vbar<6>(); h_issue<W_NT2, X_NT2>(H, KROT(2), 2);
      vbar<6>(); h_issue<W_NT2, X_NT2>(H, KROT(3), 3); h_issue<W_NT2, X_NT2>(H, KROT(4), 0);
#pragma unroll 1
      for (int s = 1; s < 28; ++s) { vbar<12>();
        if (((s + 4) & (RB - 1)) < NTN) h_issue<W_NT, X_NT>(H, KROT(s + 4), s & 3); else h_issue<W_NT2, X_NT2>(H, KROT(s + 4), s & 3); }
      vbar<12>(); h_kv(A, 0);
      vbar<10>(); h_kv(A, 1);
      vbar<8>();
      bar(); h_kv(A, 2);
      bar();
      { u64 seed; asm volatile("s_load_dwordx2 %0, %1, 0x0\n\ts_waitcnt lgkmcnt(0)" : "=s"(seed) : "s"(fl) : "memory"); ep = mix64(seed); }
      bar();
      vbar<8>(); h_kv(A, 3); h_kv(A, 4);
#pragma unroll 1
      for (int n = 0; n < 13; ++n) {
        vbar<8>();
        if (n == 8) h_ostore(L + OS0OFF + hw * XST, Ob + (h0 * 4 + hw) * XST, lane);
        if (PF && n >= 2 && n < 10) {
          const int r0 = ((n - 2) * 4 + (b >> 1)) * 32 + hw * 8;
          const char* src = wo + (size_t)(r0 + (r0 >> 9) * 1536) * 2048;
#pragma unroll
          for (int i = 0; i < 4; ++i) glds<PF_NT>(src + i * 4096, (lane >> 5) * 2048 + (lane & 31) * 16, lds0 + PFOFF + hw * 1024);
        }
        h_kv(A, 2 * n + 5); h_kv(A, 2 * n + 6);
        if (n == 9 && tid == 256) __hip_atomic_store(oflag + c, ep, RLX_AGENT);
      }
      vbar<8>(); h_kv(A, 31);
      vbar<4>();
      wait_flags4(oflag, ep, lane);
      bar();
    } else {
      const int r4 = ((b >> 1) + c) & 3;
#define KROT4(s) (((s) & ~3) | (((s) + r4) & 3))
      HG2 H; h_setup128(H, x, wq + h0 * 256, lds0, hw, lane);
      h_issue128<W_NT, X_NT>(H, KROT4(0), 0); h_issue128<W_NT2, X_NT2>(H, KROT4(1), 1);
      { u64 seed; asm volatile("s_load_dwordx2 %0, %1, 0x0\n\ts_waitcnt lgkmcnt(0)" : "=s"(seed) : "s"(fl) : "memory"); ep = mix64(seed); }
#pragma unroll 1
      for (int ph = 0; ph < 2; ++ph) {
        vbar<8>();
        h_issue128<W_NT2, X_NT2>(H, KROT4(2), 2);
        if (ph) h_ostore(L + hw * XST, Ob + (h0 * 4 + hw) * XST, lane);
        int sl = 0;
#pragma unroll 1
        for (int s = 0; s < 13; ++s) {
          vbar<8>();
          if (((s + 3) & 3) == 0) h_issue128<W_NT, X_NT>(H, KROT4(s + 3), sl); else h_issue128<W_NT2, X_NT2>(H, KROT4(s + 3), sl);
          sl = sl == 2 ? 0 : sl + 1;
          if (ph && s == 2 && tid == 256) __hip_atomic_store(oflag + c, ep, RLX_AGENT);
        }
        vbar<8>(); h_kv(A, 0);
        vbar<4>(); h_kv(A, 1);
        bar();
        bar(); h_kv(A, 2);
        bar();
        vbar<8>(); h_kv(A, 3); h_kv(A, 4);
#pragma unroll 1
        for (int n = 0; n < 5; ++n) { vbar<8>();
          if (PF && ph == 0 && n < 4) {
            const char* srcw = wq + h1 * 256 + (size_t)((n * 4 + (b >> 1)) * 64 + hw * 16) * 8192;
#pragma unroll
            for (int i = 0; i < 4; ++i) glds<PF_NT>(srcw + (size_t)i * 4 * 8192, (lane >> 4) * 8192 + (lane & 15) * 16, lds0 + PFOFF + hw * 1024);
            const char* srcx = x + (size_t)(28 * c + 7 * hw) * 2048 + n * 512;
#pragma unroll
            for (int i = 0; i < 4; ++i) glds<0>(srcx + (i < 3 ? i : 2) * 4096 + (i == 3 ? 2048 : 0), (lane >> 5) * 2048 + (lane & 31) * 16, lds0 + PFOFF + hw * 1024);
          }
          h_kv(A, 2 * n + 5); h_kv(A, 2 * n + 6); }
        vbar<8>(); h_kv(A, 15);
        vbar<4>();
        if (ph == 0) {
          H.w += 16 * 256;
          vbar<0>();
          h_issue128<W_NT, X_NT>(H, KROT4(0), 0); h_issue128<W_NT2, X_NT2>(H, KROT4(1), 1);
          A.k0 += 16 * 131072; A.v0 += 16 * 131072;
        } else {
          wait_flags4(oflag, ep, lane);
          bar();
        }
      }
    }
  }
  if (wid < 4) {
    f32x4 acc[7][4];
    c_gemm(acc, L, wid, lane, true);
    STAMP(5);
    lbar();
    if (wid != j) {
#pragma unroll
      for (int mt = 0; mt < 7; ++mt)
#pragma unroll
        for (int e = 0; e < 4; e += 2) { const u32x4 pk = {cvtpk(acc[mt][e][0], acc[mt][e][1]), cvtpk(acc[mt][e][2], acc[mt][e][3]), cvtpk(acc[mt][e + 1][0], acc[mt][e + 1][1]), cvtpk(acc[mt][e + 1][2], acc[mt][e + 1][3])};
          __builtin_amdgcn_raw_buffer_store_b128(pk, prs, lane * 16 + ((wid * 4 + j) * 28 + mt * 2 + (e >> 1)) * 1024, 0, 16); }
    } else {
#pragma unroll
      for (int mt = 0; mt < 7; ++mt)
#pragma unroll
        for (int e = 0; e < 4; ++e) *(LAS f32x4*)(L + QXOFF + (mt * 4 + e) * 1024 + lane * 16) = acc[mt][e];
    }
    vlbar();
    STAMP(6);
    bar();
    STAMP(7);
  } else {
    int hz = hw, lz = lane; asm volatile("" : "+s"(hz)); asm volatile("" : "+v"(lz));
    HG H; h_setup(H, Ob + j * 16 * XST, 0, wo, wo + 256, 2048, lds0, hz, lz);
    u64 fv = 0;
    h_issue<W_NT, 2>(H, KROT(0), 0); h_issue<W_NT, 2>(H, KROT(1), 1);
    vbar<6>();
    h_issue<W_NT2, 2>(H, KROT(2), 2);
    h_ostore(L + (grpB ? 0 : OS1OFF) + hz * XST, Ob + (h1 * 4 + hz) * XST, lz);
    vbar<13>(); h_issue<W_NT2, 2>(H, KROT(3), 3); h_issue<W_NT2, 2>(H, KROT(4), 0);
#pragma unroll 1
    for (int s = 1; s < 28; ++s) {
      vbar<12>();
      if (s == 9) asm volatile("global_load_dwordx2 %0, %1, %2 sc1" : "=v"(fv) : "v"((lz & 3) * 8 + 32), "s"(oflag) : "memory");
      if (s == 12) { asm volatile("" : "+v"(fv)); if (!__all(fv == ep)) wait_flags4(oflag + 4, ep, lz); }
      const int n = s + 4, kb = (n >> 4) * 48 + KROT(n);
      if ((n & (RB - 1)) < NTN) h_issue<W_NT, 2>(H, kb, s & 3); else h_issue<W_NT2, 2>(H, kb, s & 3);
      if (s == 1 && tid == 256) __hip_atomic_store(oflag + 4 + c, ep, RLX_AGENT);
    }
    vbar<12>(); vbar<6>(); vbar<0>(); bar();
    bar();
    bar();
    if (hw == 0) { if (lane == 0) __hip_atomic_store(pflag + j, ep, RLX_AGENT); wait_flags4(pflag, ep, lane); }
    vbar<0>();
    if (bid == 0 && hw == 3) {
#pragma unroll 1
      for (unsigned spins = 0; spins < (1u << 17); ++spins) {
        bool ok = true;
#pragma unroll
        for (int k = 0; k < 4; ++k) ok &= __hip_atomic_load(fl + 512 + lane * 8 + 4 + k, RLX_AGENT) == ep;
        if (__all(ok)) break;
        __builtin_amdgcn_s_sleep(2);
      }
      if (lane == 0) __hip_atomic_store(fl, ep, RLX_AGENT);
    }
  }
  if (wid < 7) {
    f32x4 in[3][4];
#pragma unroll
    for (int k = 0; k < 3; ++k) { const int src = (j + 1 + k) & 3;
#pragma unroll
      for (int e = 0; e < 4; e += 2) { const u32x4 pk = __builtin_amdgcn_raw_buffer_load_b128(prs, lane * 16, ((j * 4 + src) * 28 + wid * 2 + (e >> 1)) * 1024, 16);
        in[k][e] = (f32x4){__uint_as_float(pk[0] << 16), __uint_as_float(pk[0] & 0xffff0000u), __uint_as_float(pk[1] << 16), __uint_as_float(pk[1] & 0xffff0000u)};
        in[k][e + 1] = (f32x4){__uint_as_float(pk[2] << 16), __uint_as_float(pk[2] & 0xffff0000u), __uint_as_float(pk[3] << 16), __uint_as_float(pk[3] & 0xffff0000u)}; }
    }
    const int q = lane & 15, g = lane >> 4;
    f32x4 sum[4];
#pragma unroll
    for (int e = 0; e < 4; ++e) sum[e] = *(LAS const f32x4*)(L + QXOFF + (wid * 4 + e) * 1024 + lane * 16) + in[0][e] + (in[1][e] + in[2][e]);
#pragma unroll
    for (int pr = 0; pr < 2; ++pr)
      *(GAS u32x4*)(out + ((size_t)(b * 112 + 16 * wid + q) * 2048 + br * 1024 + c * 256 + j * 64 + pr * 32 + g * 8) * 2) = pack32(sum[2 * pr], sum[2 * pr + 1]);
  }
  STAMP(8); CSTAMP(1);
}

extern "C" void kernel_launch(void* const* d_in, const int* in_sizes, int n_in, void* d_out, int out_size, void* d_ws, size_t ws_size, hipStream_t stream) {
  (void)in_sizes; (void)n_in; (void)out_size; (void)ws_size;
  (void)hipFuncSetAttribute((const void*)fused, hipFuncAttributeMaxDynamicSharedMemorySize, LDS_BYTES);
  fused<<<256, 512, LDS_BYTES, stream>>>((const char*)d_in[0], (const char*)d_in[1], (const char*)d_in[2], (const char*)d_in[3], (const char*)d_in[4],
                                         (const char*)d_in[5], (const char*)d_in[6], (const char*)d_in[7], (const char*)d_in[8], (const char*)d_in[9], (char*)d_ws, (char*)d_out);
}
```

```cpp
#include <hip/hip_runtime.h>
#include <hip/hip_bf16.h>
#include <stdint.h>

typedef __attribute__((ext_vector_type(8))) short bf16x8;
typedef __attribute__((ext_vector_type(4))) short s16x4;
typedef __attribute__((ext_vector_type(4))) float f32x4;
typedef __attribute__((ext_vector_type(2))) float f32x2;
typedef __attribute__((ext_vector_type(16))) float f32x16;
typedef __attribute__((ext_vector_type(4))) unsigned u32x4;
typedef __attribute__((ext_vector_type(2))) __bf16 bf16x2_t;
typedef short v4i16_t __attribute__((ext_vector_type(4)));
typedef unsigned long long u64;
#define LAS __attribute__((address_space(3)))
#define GAS __attribute__((address_space(1)))
#define DI __device__ __forceinline__
#define MFMA16(a, b, c) __builtin_amdgcn_mfma_f32_16x16x32_bf16((a), (b), (c), 0, 0, 0)
#define MFMA32(a, b, c) __builtin_amdgcn_mfma_f32_32x32x16_bf16((a), (b), (c), 0, 0, 0)
#define SBAR() __builtin_amdgcn_sched_barrier(0)
#define RLX_AGENT __ATOMIC_RELAXED, __HIP_MEMORY_SCOPE_AGENT

#define W_NT 0
#define W_NT2 0
#define RB 8
#define PBF16 1
#define STAGGER 1
#define PF 0
#define PF_NT 0
#define X_NT 0
#define X_NT2 0
#define KV_NT 1
#define WARMUP 0
#define REVA 0
#define NTN 2
#define ROT 1
#define THR 8.0f

constexpr int LDS_BYTES = 163840;
constexpr int SLOT = 16384;
constexpr int WOFF = 65536, WST = 16384;
constexpr int XOFF = 131072, XST = 7168;
constexpr int QXOFF = 65536;
constexpr int Q1OFF = 131072;
constexpr int Q0OFF = 98304;
constexpr int OS0OFF = Q0OFF, OS1OFF = 2 * SLOT;
constexpr int PFOFF = 159744;
constexpr int NSL = 6;
constexpr size_t WS_O = 0;
constexpr size_t WS_PART = 16u << 20;
constexpr size_t PART_TILE = 16 * 28 * 1024;
constexpr size_t WS_FLAG = 64u << 20;

template <int NT>
DI void glds(const void* sbase, unsigned voff, unsigned ldsdst) {
  if (NT == 1) asm volatile("s_mov_b32 m0, %2\n\ts_nop 3\n\tglobal_load_lds_dwordx4 %0, %1 nt" ::"v"(voff), "s"(sbase), "s"(ldsdst) : "memory");
  else if (NT == 2) asm volatile("s_mov_b32 m0, %2\n\ts_nop 3\n\tglobal_load_lds_dwordx4 %0, %1 sc1" ::"v"(voff), "s"(sbase), "s"(ldsdst) : "memory");
  else asm volatile("s_mov_b32 m0, %2\n\ts_nop 3\n\tglobal_load_lds_dwordx4 %0, %1" ::"v"(voff), "s"(sbase), "s"(ldsdst) : "memory");
}
DI void touch(const char* addr, unsigned ldsdst) {
  asm volatile("s_mov_b32 m0, %1\n\ts_nop 3\n\tglobal_load_lds_dword %0, off" ::"v"(addr), "s"(ldsdst) : "memory");
}
template <int N> DI void vbar() { asm volatile("s_waitcnt vmcnt(%0)\n\ts_barrier" ::"i"(N) : "memory"); }
template <int N> DI void wait_vm() { asm volatile("s_waitcnt vmcnt(%0)" ::"i"(N) : "memory"); }
DI void lbar() { asm volatile("s_waitcnt lgkmcnt(0)\n\ts_barrier" ::: "memory"); }
DI void bar() { asm volatile("s_barrier" ::: "memory"); }
DI unsigned cvtpk(float lo, float hi) { const f32x2 v = {lo, hi}; return __builtin_bit_cast(unsigned, __builtin_convertvector(v, bf16x2_t)); }
#define STAMP(i) do {} while (0)
#define CSTAMP(i) do {} while (0)
#define HSTAMP(i) do {} while (0)
DI s16x4 trread(LAS const char* p) { return __builtin_bit_cast(s16x4, __builtin_amdgcn_ds_read_tr16_b64_v4i16((LAS v4i16_t*)p)); }
DI bf16x8 ldsr(LAS const char* p) { return *(LAS const bf16x8*)p; }
DI bf16x8 cat8(s16x4 a, s16x4 b) { return __builtin_shufflevector(a, b, 0, 1, 2, 3, 4, 5, 6, 7); }
DI int fsw(int row) { return (row & 3) | ((((row >> 2) ^ (row >> 3)) & 1) << 2); }
DI u64 mix64(u64 z) { z += 0x9E3779B97F4A7C15ull; z = (z ^ (z >> 30)) * 0xBF58476D1CE4E5B9ull; z = (z ^ (z >> 27)) * 0x94D049BB133111EBull; return z ^ (z >> 31); }

DI u32x4 pack32(f32x4 a, f32x4 b) {
  auto r0 = __builtin_amdgcn_permlane32_swap(cvtpk(a[0], a[1]), cvtpk(b[0], b[1]), false, false);
  auto r1 = __builtin_amdgcn_permlane32_swap(cvtpk(a[2], a[3]), cvtpk(b[2], b[3]), false, false);
  auto s0 = __builtin_amdgcn_permlane16_swap(r0[0], r0[1], false, false);
  auto s1 = __builtin_amdgcn_permlane16_swap(r1[0], r1[1], false, false);
  u32x4 o = {s0[0], s1[0], s0[1], s1[1]};
  return o;
}
DI float rowmax4(float mx) {
  auto r = __builtin_amdgcn_permlane32_swap(__float_as_uint(mx), __float_as_uint(mx), false, false);
  mx = __builtin_fmaxf(__uint_as_float(r[0]), __uint_as_float(r[1]));
  auto r2 = __builtin_amdgcn_permlane16_swap(__float_as_uint(mx), __float_as_uint(mx), false, false);
  return __builtin_fmaxf(__uint_as_float(r2[0]), __uint_as_float(r2[1]));
}
DI float max4(float m, const f32x4& c) { return __builtin_fmaxf(__builtin_fmaxf(__builtin_fmaxf(m, c[0]), c[1]), __builtin_fmaxf(c[2], c[3])); }

template <bool VM>
DI void c_stage(f32x4 (&acc)[7][4], bf16x8 (&X)[7], const bf16x8 (&W)[4], bf16x8 (&Wn)[4], LAS const char* xr, LAS const char* const (&wr)[8], int cs, int ns) {
  s16x4 lo[4];
#pragma unroll
  for (int gp = 0; gp < 28; ++gp) {
    const int mt = gp >> 2, e = gp & 3;
    acc[mt][e] = MFMA16(W[e], X[mt], acc[mt][e]);
    SBAR();
    if (gp == 0) X[6] = ldsr(xr + cs * XST + 6 * 1024);
    if (gp == 5) { if (VM) asm volatile("s_waitcnt vmcnt(0) lgkmcnt(0)\n\ts_barrier" ::: "memory"); else lbar(); }
    if (gp >= 6 && gp < 22 && (gp & 1) == 0) { const int k = (gp - 6) >> 1;
      if ((k & 1) == 0) lo[k >> 1] = trread(wr[k >> 1] + ns * WST); else Wn[k >> 1] = cat8(lo[k >> 1], trread(wr[4 + (k >> 1)] + ns * WST + 1024)); }
    if (gp >= 7 && (gp & 3) == 3) X[mt - 1] = ldsr(xr + ns * XST + (mt - 1) * 1024);
    SBAR();
  }
}
DI void c_gemm(f32x4 (&acc)[7][4], LAS char* L, int wid, int lane, bool xlin) {
  asm volatile("" : "+v"(lane));
  const int q = lane & 15, g = lane >> 4;
  LAS const char* xr = L + XOFF + (xlin ? lane * 16 : q * 64 + ((g ^ ((-(q >> 2)) & 3)) << 4));
  LAS const char* wr[8];
  { const int row = 8 * g + (q >> 2), p = q & 3, f = fsw(row), nt0 = 4 * (wid & 1);
    LAS const char* wb = L + WOFF + (wid >> 1) * 8192 + row * 256 + p * 8;
#pragma unroll
    for (int e = 0; e < 4; ++e) { wr[e] = wb + (((nt0 + e) ^ f) << 5); wr[4 + e] = wb + (((nt0 + e) ^ f ^ 4) << 5); } }
#pragma unroll
  for (int mt = 0; mt < 7; ++mt)
#pragma unroll
    for (int e = 0; e < 4; ++e) acc[mt][e] = (f32x4){0.f, 0.f, 0.f, 0.f};
  bf16x8 X[7], Wa[4], Wb[4];
  lbar();
#pragma unroll
  for (int e = 0; e < 4; ++e) Wa[e] = cat8(trread(wr[e]), trread(wr[4 + e] + 1024));
#pragma unroll
  for (int mt = 0; mt < 6; ++mt) X[mt] = ldsr(xr + mt * 1024);
#pragma unroll 1
  for (int s = 0; s < 32; s += 4) {
    c_stage<false>(acc, X, Wa, Wb, xr, wr, 0, 1);
    c_stage<false>(acc, X, Wb, Wa, xr, wr, 1, 2);
    c_stage<false>(acc, X, Wa, Wb, xr, wr, 2, 3);
    c_stage<true>(acc, X, Wb, Wa, xr, wr, 3, 0);
  }
}

constexpr int DST = 30720;
DI void c_stage128(f32x4 (&acc)[7][2], bf16x8 (&X)[14], const bf16x8 (&W)[4], bf16x8 (&Wn)[4], LAS const char* (&xr)[2], LAS const char* (&wr)[4], int dn) {
  s16x4 lo[4];
#pragma unroll
  for (int gp = 0; gp < 28; ++gp) {
    const int i = gp >> 1, e = gp & 1, kh = i / 7, mt = i % 7;
    acc[mt][e] = MFMA16(W[2 * kh + e], X[i], acc[mt][e]);
    SBAR();
    if (gp < 3) X[11 + gp] = ldsr(xr[1] + (4 + gp) * 2048);
    if (gp == 3) { xr[0] += dn; wr[0] += dn; }
    if (gp == 4) { xr[1] += dn; wr[1] += dn; }
    if (gp == 5) { wr[2] += dn; wr[3] += dn; }
    if (gp == 6) lbar();
    if (gp >= 7 && (gp & 1) == 1) { const int z = (gp - 7) >> 1; X[z] = ldsr(xr[z / 7] + (z % 7) * 2048); }
    if (gp >= 8 && gp < 24 && (gp & 1) == 0) { const int k = (gp - 8) >> 1, f = k >> 1;
      if ((k & 1) == 0) lo[f] = trread(wr[f & 1] + (f >> 1) * 8192); else Wn[f] = cat8(lo[f], trread(wr[2 + (f & 1)] + (f >> 1) * 8192 + 1024)); }
    SBAR();
  }
}
DI void c_gemm128(f32x4 (&acc)[7][2], LAS char* L, int wid, int lane) {
  asm volatile("" : "+v"(lane));
  const int q = lane & 15, g = lane >> 4;
  LAS const char* xr[2] = {L + WOFF + 16384 + q * 128 + ((g ^ (q & 7)) << 4), L + WOFF + 16384 + q * 128 + (((4 + g) ^ (q & 7)) << 4)};
  LAS const char* wr[4];
  { const int row = 8 * g + (q >> 2), p = q & 3, f = fsw(row), nt0 = 2 * wid;
    LAS const char* wb = L + WOFF + row * 256 + p * 8;
#pragma unroll
    for (int e = 0; e < 2; ++e) { wr[e] = wb + (((nt0 + e) ^ f) << 5); wr[2 + e] = wb + (((nt0 + e) ^ f ^ 4) << 5); } }
#pragma unroll
  for (int mt = 0; mt < 7; ++mt) { acc[mt][0] = (f32x4){0.f, 0.f, 0.f, 0.f}; acc[mt][1] = (f32x4){0.f, 0.f, 0.f, 0.f}; }
  bf16x8 X[14], Wa[4], Wb[4];
  lbar();
#pragma unroll
  for (int f = 0; f < 4; ++f) Wa[f] = cat8(trread(wr[f & 1] + (f >> 1) * 8192), trread(wr[2 + (f & 1)] + (f >> 1) * 8192 + 1024));
#pragma unroll
  for (int i = 0; i < 11; ++i) X[i] = ldsr(xr[i / 7] + (i % 7) * 2048);
  int sl = 0;
#pragma unroll 1
  for (int s = 0; s < 16; s += 2) {
    c_stage128(acc, X, Wa, Wb, xr, wr, sl == 2 ? -2 * DST : DST); sl = sl == 2 ? 0 : sl + 1;
    c_stage128(acc, X, Wb, Wa, xr, wr, sl == 2 ? -2 * DST : DST); sl = sl == 2 ? 0 : sl + 1;
  }
}

#define PIN(x) asm volatile("" : "+v"(x))
DI float mx3(float a, float b, float c) { return __builtin_elementwise_maximum(__builtin_elementwise_maximum(a, b), c); }
DI void vlbar() { asm volatile("s_waitcnt vmcnt(0) lgkmcnt(0)\n\ts_barrier" ::: "memory"); }

struct HA { const char* k0; const char* v0; unsigned kv[4], vv[4]; unsigned d; };
DI void h_kv(const HA& A, int T) {
  const int n = T >> 1; const unsigned so = (unsigned)(n >> 3) * (16u * 131072u) + (n & 7) * SLOT;
  const unsigned d = A.d + (T % NSL) * SLOT;
  if (T & 1) {
#pragma unroll
    for (int i = 0; i < 4; ++i) glds<KV_NT>(A.v0 + so, A.vv[i], d + i * 1024);
  } else {
#pragma unroll
    for (int i = 0; i < 4; ++i) glds<KV_NT>(A.k0 + so, A.kv[i], d + i * 1024);
  }
}
DI void h_kv_setup(HA& A, const char* k0, const char* v0, int row0  , unsigned lds0, int lane) {
  A.k0 = k0 + row0 * 256; A.v0 = v0 + row0 * 256; A.d = lds0 + row0 * 256;
#pragma unroll
  for (int i = 0; i < 4; ++i) { const int row = 4 * i + (lane >> 4); A.kv[i] = row * 256 + (((lane & 15) ^ row) << 4); A.vv[i] = row * 256 + (((lane & 15) ^ ((row & 3) << 2)) << 4); }
}
DI void h_ostore(LAS const char* src  , char* dst, int lane) {
  const __amdgpu_buffer_rsrc_t ors = __builtin_amdgcn_make_buffer_rsrc(dst, 0, XST, 0x00020000);
  u32x4 v[7];
#pragma unroll
  for (int i = 0; i < 7; ++i) v[i] = *(LAS const u32x4*)(src + i * 1024 + lane * 16);
#pragma unroll
  for (int i = 0; i < 7; ++i) __builtin_amdgcn_raw_buffer_store_b128(v[i], ors, i * 1024 + lane * 16, 0, 16);
}
DI void wait_flags4(GAS u64* f, u64 e, int lane) {
#pragma unroll 1
  for (unsigned spins = 0; spins < (1u << 17); ++spins) {
    const u64 v = __hip_atomic_load(f + (lane & 3), RLX_AGENT);
    if (__all(v == e)) break;
    __builtin_amdgcn_s_sleep(1);
  }
}
DI float xhalf_max(float m) { auto r = __builtin_amdgcn_permlane32_swap(__float_as_uint(m), __float_as_uint(m), false, false); return __builtin_fmaxf(__uint_as_float(r[0]), __uint_as_float(r[1])); }
DI float xhalf_sum(float m) { auto r = __builtin_amdgcn_permlane32_swap(__float_as_uint(m), __float_as_uint(m), false, false); return __uint_as_float(r[0]) + __uint_as_float(r[1]); }
struct AttnS { f32x16 O[4], nm, Ls; bf16x8 Qf[8], kf[8], ones; unsigned w0, w1; float mx; bool big; };
#define LGKM(n) __builtin_amdgcn_s_waitcnt(0xC07F | ((n) << 8))
#define EXP2(x) __builtin_amdgcn_exp2f(x)

template <int GK>
DI void a_grp(AttnS& A, f32x16& So, f32x16& Sn, LAS const char* (&ka)[8], LAS const char* (&va)[4], int dk) {
  constexpr int NT[18] = {0, 1, 2, 1, 2, 1, 2, 1, 2, 1, 2, 1, 0, 0, 0, 0, 0, 0};
  constexpr int NB[18] = {0, 0, 0, 0, 0, 0, 0, 0, 0, 0, 0, 1, 1, 1, 2, 1, 1, 1};
  constexpr int NM[18] = {0, 0, 0, 0, 0, 0, 0, 0, 0, 0, 0, 1, 3, 3, 1, 0, 0, 0};
  u32x4 pw[2]; s16x4 vt[16]; float e0 = 0.f, e1 = 0.f, e2 = 0.f;
  pw[0][0] = A.w0; pw[0][1] = A.w1;
  int it = 0, ib = 0, im = 0;
#pragma unroll
  for (int sl = 0; sl < 18; ++sl) {
    if (sl == 0) LGKM(4);
    if (sl == 4 && GK == 1) LGKM(4);
    if (sl == 9) LGKM(4);
    if (sl == 14) LGKM(3);
    if (sl < 8) Sn = MFMA32(A.kf[sl], A.Qf[sl], sl == 0 ? A.nm : Sn);
    else { const int u = (sl - 8) / 5, r = (sl - 8) % 5; const bf16x8 pf = __builtin_bit_cast(bf16x8, pw[u]);
      if (r == 0) A.Ls = MFMA32(A.ones, pf, A.Ls); else { const int z = 4 * u + r - 1; A.O[r - 1] = MFMA32(cat8(vt[2 * z], vt[2 * z + 1]), pf, A.O[r - 1]); } }
    SBAR();
    if (sl >= 1 && sl <= 11 && (sl & 1)) { const int p = 2 + (sl >> 1); unsigned w = cvtpk(So[2 * p], So[2 * p + 1]); PIN(w); pw[p >> 2][p & 3] = w; }
    if (sl == 0) { float x = EXP2(So[4]); PIN(x); So[4] = x; }
    if (sl <= 10) { float x = EXP2(So[5 + sl]); PIN(x); So[5 + sl] = x; }
    if (GK == 0 && sl == 0) { PIN(Sn); LGKM(0); bar(); }
#pragma unroll
    for (int k = 0; k < NT[sl]; ++k) { const int z = it >> 1;
      vt[it] = trread(va[z & 3] + GK * 8192 + (z >> 2) * 4096 + (it & 1) * 2048); ++it; }
    if (GK == 0 && sl >= 1 && sl <= 8) { ka[sl - 1] += dk; PIN(ka[sl - 1]); }
#pragma unroll
    for (int k = 0; k < NB[sl]; ++k) { A.kf[ib] = ldsr(ka[ib] + GK * 8192); ++ib; }
#pragma unroll
    for (int k = 0; k < NM[sl]; ++k) { float x = mx3(im == 0 ? Sn[0] : A.mx, Sn[2 * im], Sn[2 * im + 1]); PIN(x); A.mx = x; ++im; }
    if (sl == 14) A.big = __any(A.mx > THR);
    if (GK == 1 && sl >= 12 && sl <= 15) { va[sl - 12] += dk; PIN(va[sl - 12]); }
    if (sl == 15) { e0 = EXP2(Sn[0]); e1 = EXP2(Sn[1]); PIN(e0); PIN(e1); }
    if (sl == 16) { unsigned w = cvtpk(e0, e1); PIN(w); A.w0 = w; e2 = EXP2(Sn[2]); PIN(e2); }
    if (sl == 17) { unsigned w = cvtpk(e2, EXP2(Sn[3])); PIN(w); A.w1 = w; }
    SBAR();
  }
}
DI void a_check(AttnS& A, f32x16& Sn, bool first) {
  if (first || A.big) {
    float d = xhalf_max(A.mx);
    if (!first) d = __builtin_fmaxf(d, 0.f);
    const float f = first ? 0.f : EXP2(-d);
    A.nm -= d; Sn -= d;
#pragma unroll
    for (int dt = 0; dt < 4; ++dt) A.O[dt] *= f;
    A.Ls *= f;
    A.w0 = cvtpk(EXP2(Sn[0]), EXP2(Sn[1])); A.w1 = cvtpk(EXP2(Sn[2]), EXP2(Sn[3]));
  }
}
DI void a_loadq(AttnS& A, LAS const char* Qi, int wid, int lane) {
  const int q = lane & 31, h = lane >> 5; int mt = 2 * wid + (q >> 4); mt = mt > 6 ? 6 : mt;
  LAS const char* p = Qi + mt * 4096 + (h * 16 + (q & 15)) * 16;
#pragma unroll
  for (int s = 0; s < 8; ++s) A.Qf[s] = ldsr(p + (s >> 1) * 1024 + (s & 1) * 512);
#pragma unroll
  for (int i = 0; i < 16; ++i) A.nm[i] = 0.f;
}
DI void c_attn(LAS char* L, int wid, int lane, int nper  ) {
  asm volatile("" : "+v"(lane));
  const int r = lane & 31, h = lane >> 5;
  LAS const char* ka[8]; LAS const char* va[4];
#pragma unroll
  for (int s = 0; s < 8; ++s) ka[s] = L + r * 256 + (((h ^ (r & 15)) << 4) ^ (s << 5));
  { const int db = (lane >> 4) & 1, qq = (lane & 15) >> 2, p = lane & 3;
#pragma unroll
    for (int dt = 0; dt < 4; ++dt) va[dt] = L + SLOT + (4 * h + qq) * 256 + (((2 * dt + db) ^ (qq << 1)) << 5) + p * 8; }
  AttnS A; f32x16 SA, SB;
#pragma unroll
  for (int dt = 0; dt < 4; ++dt)
#pragma unroll
    for (int i = 0; i < 16; ++i) A.O[dt][i] = 0.f;
#pragma unroll
  for (int i = 0; i < 16; ++i) A.Ls[i] = 0.f;
  A.ones = (bf16x8){0x3F80, 0x3F80, 0x3F80, 0x3F80, 0x3F80, 0x3F80, 0x3F80, 0x3F80}; PIN(A.ones);
  a_loadq(A, L + Q0OFF, wid, lane);
  bar();
#pragma unroll
  for (int s = 0; s < 8; ++s) SA = MFMA32(ldsr(ka[s]), A.Qf[s], s == 0 ? A.nm : SA);
#pragma unroll
  for (int s = 0; s < 8; ++s) A.kf[s] = ldsr(ka[s] + 8192);
  A.mx = SA[0];
#pragma unroll
  for (int i = 0; i < 16; i += 2) A.mx = mx3(A.mx, SA[i], SA[i + 1]);
  a_check(A, SA, true);
  int ks = 0;
#pragma unroll 1
  for (int n = 0; n < nper; ++n) {
    const int dk = ks == 2 ? -4 * SLOT : 2 * SLOT;
    a_grp<0>(A, SA, SB, ka, va, dk);
    a_check(A, SB, false);
    if (n == 7) a_loadq(A, L + Q1OFF, wid, lane);
    a_grp<1>(A, SB, SA, ka, va, dk);
    if ((n & 7) == 7) {
      const float inv = __builtin_amdgcn_rcpf(A.Ls[0]);
      LAS char* os = L + (nper == 8 ? 0 : (n == 7 ? OS0OFF : OS1OFF)) + 2 * wid * 1024 + ((r >> 4) * 64 + h * 16 + (r & 15)) * 16;
      if (wid < 3 || r < 16) {
#pragma unroll
        for (int dt = 0; dt < 4; ++dt)
#pragma unroll
          for (int g = 0; g < 4; g += 2) {
            auto x0 = __builtin_amdgcn_permlane32_swap(cvtpk(A.O[dt][4 * g] * inv, A.O[dt][4 * g + 1] * inv), cvtpk(A.O[dt][4 * g + 4] * inv, A.O[dt][4 * g + 5] * inv), false, false);
            auto x1 = __builtin_amdgcn_permlane32_swap(cvtpk(A.O[dt][4 * g + 2] * inv, A.O[dt][4 * g + 3] * inv), cvtpk(A.O[dt][4 * g + 6] * inv, A.O[dt][4 * g + 7] * inv), false, false);
            const u32x4 o = {x0[0], x1[0], x0[1], x1[1]};
            *(LAS u32x4*)(os + dt * XST + g * 256) = o;
          }
      }
    }
    if (n < nper - 1) a_check(A, SA, n == 7);
    ks = ks == 2 ? 0 : ks + 1;
  }
}

struct HG { const char* w; const char* x; unsigned ldw, xstep; unsigned wv[4], xv[2]; unsigned wd, xd, xd1; };
DI void h_setup(HG& H, const char* x, unsigned ldx, const char* w0, const char* w1, unsigned ldw, unsigned lds0, int hw, int lane) {
  H.w = ((hw >> 1) ? w1 : w0) + (size_t)(16 * (hw & 1)) * ldw; H.ldw = ldw;
#pragma unroll
  for (int i = 0; i < 4; ++i) { const int row = 4 * i + (lane >> 4); H.wv[i] = row * ldw + (((lane & 15) ^ (fsw(row) << 1)) << 4); }
  if (ldx) {
    H.x = x + (size_t)(32 * hw) * ldx; H.xstep = 64;
    const unsigned xo = (lane >> 2) * ldx + (((lane & 3) ^ ((-(lane >> 4)) & 3)) << 4);
    H.xv[0] = xo; H.xv[1] = xo + (hw < 3 ? 16 * ldx : 0);
  } else {
    H.x = x + hw * 2048; H.xstep = XST; H.xv[0] = lane * 16; H.xv[1] = lane * 16 + (hw < 3 ? 1024 : 0);
  }
  H.wd = lds0 + WOFF + hw * 4096; H.xd = lds0 + XOFF + hw * 2048; H.xd1 = hw < 3 ? 1024 : 0;
}
template <int NT, int XP>
DI void h_issue(const HG& H, int kb  , int slot) {
  const char* w = H.w + (size_t)kb * 32 * H.ldw; const char* x = H.x + kb * H.xstep;
#pragma unroll
  for (int i = 0; i < 4; ++i) glds<NT>(w, H.wv[i], H.wd + slot * WST + i * 1024);
  glds<XP>(x, H.xv[0], H.xd + slot * XST); glds<XP>(x, H.xv[1], H.xd + slot * XST + H.xd1);
}
struct HG2 { const char* w; const char* x; unsigned wv[4], xv[4]; unsigned wd, xd, xd3; };
DI void h_setup128(HG2& H, const char* x, const char* w, unsigned lds0, int hw, int lane) {
  H.w = w + (size_t)(16 * hw) * 8192;
#pragma unroll
  for (int i = 0; i < 4; ++i) { const int row = 4 * i + (lane >> 4); H.wv[i] = row * 8192 + (((lane & 15) ^ (fsw(row) << 1)) << 4); }
  const int p0 = hw < 2 ? 4 * hw : 8 + 3 * (hw - 2);
  H.x = x + (size_t)(8 * p0) * 2048;
  H.xv[0] = (lane >> 3) * 2048 + (((lane & 7) ^ ((lane >> 3) & 7)) << 4);
  H.wd = lds0 + WOFF + hw * 4096; H.xd = lds0 + WOFF + 16384 + p0 * 1024; H.xd3 = hw < 2 ? 0u : lds0 + PFOFF + hw * 256;
}
template <int NT, int XP>
DI void h_issue128(const HG2& H, int kd  , int slot) {
  const char* w = H.w + (size_t)kd * 64 * 8192; const char* x = H.x + kd * 128;
#pragma unroll
  for (int i = 0; i < 4; ++i) glds<NT>(w, H.wv[i], H.wd + slot * DST + i * 1024);
#pragma unroll
  for (int i = 0; i < 3; ++i) glds<XP>(x + i * 16384, H.xv[0], H.xd + slot * DST + i * 1024);
  if (H.xd3 == 0) glds<XP>(x + 3 * 16384, H.xv[0], H.xd + slot * DST + 3072);
  else asm volatile("s_mov_b32 m0, %2\n\ts_nop 3\n\tglobal_load_lds_dword %0, %1" ::"v"(0u), "s"(x), "s"(H.xd3) : "memory");
}

__global__ __launch_bounds__(512, 2) void fused(const char* xA, const char* kA, const char* vA, const char* wqA, const char* woA,
                                                const char* xB, const char* kB, const char* vB, const char* wqB, const char* woB, char* ws, char* out) {
  const char* pc0; asm volatile("s_getpc_b64 %0" : "=s"(pc0));
  extern __shared__ __attribute__((aligned(16))) char lds[];
  LAS char* L = (LAS char*)lds; const unsigned lds0 = (unsigned)(uintptr_t)L;
  const int tid = threadIdx.x, lane = tid & 63; const int wid = __builtin_amdgcn_readfirstlane(tid >> 6);
  const int bid = blockIdx.x, xcd = bid & 7, idx = bid >> 3, br = xcd >> 2, j = xcd & 3, b = idx >> 2, c = idx & 3, h0 = 4 * j + c, h1 = h0 + 16;
  char* Ob = ws + WS_O + (size_t)(br * 8 + b) * (128 * XST);
  const int tile = (br * 8 + b) * 4 + c;
  const __amdgpu_buffer_rsrc_t prs = __builtin_amdgcn_make_buffer_rsrc(ws + WS_PART + (size_t)tile * PART_TILE, 0, (int)PART_TILE, 0x00020000);
  STAMP(0); CSTAMP(0);
  const int hw = wid & 3;
  const char* kc = (br ? kB : kA) + (size_t)(b * 32 + h0) * 131072; const char* vc = (br ? vB : vA) + (size_t)(b * 32 + h0) * 131072;
  GAS u64* fl = (GAS u64*)(ws + WS_FLAG);
  GAS u64* oflag = fl + 512 + ((br * 8 + b) * 4 + j) * 8; GAS u64* pflag = fl + 2048 + tile * 8;
  const int r8 = RB == 16 ? (2 * b + 4 * c) & 15 : RB == 8 ? (b + 2 * c) & 7 : (b + c) & (RB - 1);
#define KROT(s) (((s) & ~(RB - 1)) | (((s) + r8) & (RB - 1)))
#define KRQ(s) (REVA ? 31 - KROT(s) : KROT(s))
  u64 ep = 0;
  const char* wo = (br ? woB : woA) + (size_t)(512 * j) * 2048 + c * 512;
  const bool grpB = STAGGER && (b & 1);
  const float cs = 0.08838834764831845f * 1.4426950408889634f;
  if (wid < 4) {
    if ((WARMUP & 1) && wid == 0) {
      const char* a;
      if (lane < 16) a = ((lane & 1) ? vc : kc) + (size_t)((lane >> 1) & 1) * (16u * 131072u) + (lane >> 2) * 32768;
      else if (lane < 48) { const int r = (lane - 16) * 32; a = wo + (size_t)(r + (r >> 9) * 1536) * 2048; }
      else if (lane < 63) a = ws + WS_PART + (size_t)tile * PART_TILE + (lane - 48) * 28672 + 20480;
      else a = out + (size_t)(b * 112) * 4096;
      touch(a, lds0 + PFOFF);
    }
    if ((WARMUP & 2) && wid >= 1) {
#pragma unroll
      for (int k = 0; k < 2; ++k) touch(pc0 + (((wid - 1) * 2 + k) * 64 + lane) * 64, lds0 + PFOFF + 256 * wid);
    }
#pragma unroll 1
    for (int ph = 0; ph < (grpB ? 2 : 1); ++ph) {
      int lz = lane; asm volatile("" : "+v"(lz));
      if (grpB) {
        f32x4 acc[7][2];
        c_gemm128(acc, L, wid, lane);
        lbar();
#pragma unroll
        for (int mt = 0; mt < 7; ++mt) *(LAS u32x4*)(L + Q0OFF + (mt * 4 + wid) * 1024 + lz * 16) = pack32(acc[mt][0] * cs, acc[mt][1] * cs);
      } else {
        f32x4 acc[7][4];
        c_gemm(acc, L, wid, lane, false);
        lbar();
        LAS char* qi = L + ((wid >> 1) ? Q1OFF : Q0OFF) + 2 * (wid & 1) * 1024 + lz * 16;
#pragma unroll
        for (int mt = 0; mt < 7; ++mt)
#pragma unroll
          for (int e = 0; e < 2; ++e) *(LAS u32x4*)(qi + (mt * 4 + e) * 1024) = pack32(acc[mt][2 * e] * cs, acc[mt][2 * e + 1] * cs);
      }
      lbar();
      STAMP(1 + ph);
      c_attn(L, wid, lane, grpB ? 8 : 16);
      if (ph == 0) STAMP(3);
    }
    STAMP(4);
  } else {
    const char* x = (br ? xB : xA) + (size_t)b * 112 * 2048; const char* wq = br ? wqB : wqA;
    HA A; h_kv_setup(A, kc, vc, 16 * hw, lds0, lane);
    if (!grpB) {
      HG H; h_setup(H, x, 2048, wq + h0 * 256, wq + h1 * 256, 8192, lds0, hw, lane);
      h_issue<W_NT, X_NT>(H, KRQ(0), 0); h_issue<W_NT, X_NT>(H, KRQ(1), 1);
      vbar<6>(); h_issue<W_NT2, X_NT2>(H, KRQ(2), 2);
      vbar<6>(); h_issue<W_NT2, X_NT2>(H, KRQ(3), 3); h_issue<W_NT2, X_NT2>(H, KRQ(4), 0);
#pragma unroll 1
      for (int s = 1; s < 28; ++s) { vbar<12>();
        if (((s + 4) & (RB - 1)) < NTN) h_issue<W_NT, X_NT>(H, KRQ(s + 4), s & 3); else h_issue<W_NT2, X_NT2>(H, KRQ(s + 4), s & 3); }
      vbar<12>(); h_kv(A, 0);
      vbar<10>(); h_kv(A, 1);
      vbar<8>();
      bar(); h_kv(A, 2);
      bar();
      { u64 seed; asm volatile("s_load_dwordx2 %0, %1, 0x0\n\ts_waitcnt lgkmcnt(0)" : "=s"(seed) : "s"(fl) : "memory"); ep = mix64(seed); }
      bar();
      vbar<8>(); h_kv(A, 3); h_kv(A, 4);
#pragma unroll 1
      for (int n = 0; n < 13; ++n) {
        vbar<8>();
        if (n == 8) h_ostore(L + OS0OFF + hw * XST, Ob + (h0 * 4 + hw) * XST, lane);
        if (PF && n >= 2 && n < 10) {
          const int r0 = ((n - 2) * 4 + (b >> 1)) * 32 + hw * 8;
          const char* src = wo + (size_t)(r0 + (r0 >> 9) * 1536) * 2048;
#pragma unroll
          for (int i = 0; i < 4; ++i) glds<PF_NT>(src + i * 4096, (lane >> 5) * 2048 + (lane & 31) * 16, lds0 + PFOFF + hw * 1024);
        }
        h_kv(A, 2 * n + 5); h_kv(A, 2 * n + 6);
        if (n == 9 && tid == 256) __hip_atomic_store(oflag + c, ep, RLX_AGENT);
      }
      vbar<8>(); h_kv(A, 31);
      vbar<4>();
      wait_flags4(oflag, ep, lane);
      bar();
    } else {
      const int r4 = ((b >> 1) + c) & 3;
#define KROT4(s) (((s) & ~3) | (((s) + r4) & 3))
      HG2 H; h_setup128(H, x, wq + h0 * 256, lds0, hw, lane);
      h_issue128<W_NT, X_NT>(H, KROT4(0), 0); h_issue128<W_NT2, X_NT2>(H, KROT4(1), 1);
      { u64 seed; asm volatile("s_load_dwordx2 %0, %1, 0x0\n\ts_waitcnt lgkmcnt(0)" : "=s"(seed) : "s"(fl) : "memory"); ep = mix64(seed); }
#pragma unroll 1
      for (int ph = 0; ph < 2; ++ph) {
        vbar<8>();
        h_issue128<W_NT2, X_NT2>(H, KROT4(2), 2);
        if (ph) h_ostore(L + hw * XST, Ob + (h0 * 4 + hw) * XST, lane);
        int sl = 0;
#pragma unroll 1
        for (int s = 0; s < 13; ++s) {
          vbar<8>();
          if (((s + 3) & 3) == 0) h_issue128<W_NT, X_NT>(H, KROT4(s + 3), sl); else h_issue128<W_NT2, X_NT2>(H, KROT4(s + 3), sl);
          sl = sl == 2 ? 0 : sl + 1;
          if (ph && s == 2 && tid == 256) __hip_atomic_store(oflag + c, ep, RLX_AGENT);
        }
        vbar<8>(); h_kv(A, 0);
        vbar<4>(); h_kv(A, 1);
        bar();
        bar(); h_kv(A, 2);
        bar();
        vbar<8>(); h_kv(A, 3); h_kv(A, 4);
#pragma unroll 1
        for (int n = 0; n < 5; ++n) { vbar<8>();
          if (PF && ph == 0 && n < 4) {
            const char* srcw = wq + h1 * 256 + (size_t)((n * 4 + (b >> 1)) * 64 + hw * 16) * 8192;
#pragma unroll
            for (int i = 0; i < 4; ++i) glds<PF_NT>(srcw + (size_t)i * 4 * 8192, (lane >> 4) * 8192 + (lane & 15) * 16, lds0 + PFOFF + hw * 1024);
            const char* srcx = x + (size_t)(28 * c + 7 * hw) * 2048 + n * 512;
#pragma unroll
            for (int i = 0; i < 4; ++i) glds<0>(srcx + (i < 3 ? i : 2) * 4096 + (i == 3 ? 2048 : 0), (lane >> 5) * 2048 + (lane & 31) * 16, lds0 + PFOFF + hw * 1024);
          }
          h_kv(A, 2 * n + 5); h_kv(A, 2 * n + 6); }
        vbar<8>(); h_kv(A, 15);
        vbar<4>();
        if (ph == 0) {
          H.w += 16 * 256;
          vbar<0>();
          h_issue128<W_NT, X_NT>(H, KROT4(0), 0); h_issue128<W_NT2, X_NT2>(H, KROT4(1), 1);
          A.k0 += 16 * 131072; A.v0 += 16 * 131072;
        } else {
          wait_flags4(oflag, ep, lane);
          bar();
        }
      }
    }
  }
  if (wid < 4) {
    f32x4 acc[7][4];
    c_gemm(acc, L, wid, lane, true);
    STAMP(5);
    lbar();
    if (wid != j) {
#pragma unroll
      for (int mt = 0; mt < 7; ++mt)
#pragma unroll
        for (int e = 0; e < 4; e += 2) { const u32x4 pk = {cvtpk(acc[mt][e][0], acc[mt][e][1]), cvtpk(acc[mt][e][2], acc[mt][e][3]), cvtpk(acc[mt][e + 1][0], acc[mt][e + 1][1]), cvtpk(acc[mt][e + 1][2], acc[mt][e + 1][3])};
          __builtin_amdgcn_raw_buffer_store_b128(pk, prs, lane * 16 + ((wid * 4 + j) * 28 + mt * 2 + (e >> 1)) * 1024, 0, 16); }
    } else {
#pragma unroll
      for (int mt = 0; mt < 7; ++mt)
#pragma unroll
        for (int e = 0; e < 4; ++e) *(LAS f32x4*)(L + QXOFF + (mt * 4 + e) * 1024 + lane * 16) = acc[mt][e];
    }
    vlbar();
    STAMP(6);
    bar();
    STAMP(7);
  } else {
    int hz = hw, lz = lane; asm volatile("" : "+s"(hz)); asm volatile("" : "+v"(lz));
    HG H; h_setup(H, Ob + j * 16 * XST, 0, wo, wo + 256, 2048, lds0, hz, lz);
    u64 fv = 0;
    h_issue<W_NT, 2>(H, KROT(0), 0); h_issue<W_NT, 2>(H, KROT(1), 1);
    vbar<6>();
    h_issue<W_NT2, 2>(H, KROT(2), 2);
    h_ostore(L + (grpB ? 0 : OS1OFF) + hz * XST, Ob + (h1 * 4 + hz) * XST, lz);
    vbar<13>(); h_issue<W_NT2, 2>(H, KROT(3), 3); h_issue<W_NT2, 2>(H, KROT(4), 0);
#pragma unroll 1
    for (int s = 1; s < 28; ++s) {
      vbar<12>();
      if (s == 9) asm volatile("global_load_dwordx2 %0, %1, %2 sc1" : "=v"(fv) : "v"((lz & 3) * 8 + 32), "s"(oflag) : "memory");
      if (s == 12) { asm volatile("" : "+v"(fv)); if (!__all(fv == ep)) wait_flags4(oflag + 4, ep, lz); }
      const int n = s + 4, kb = (n >> 4) * 48 + KROT(n);
      if ((n & (RB - 1)) < NTN) h_issue<W_NT, 2>(H, kb, s & 3); else h_issue<W_NT2, 2>(H, kb, s & 3);
      if (s == 1 && tid == 256) __hip_atomic_store(oflag + 4 + c, ep, RLX_AGENT);
    }
    vbar<12>(); vbar<6>(); vbar<0>(); bar();
    bar();
    bar();
    if (hw == 0) { if (lane == 0) __hip_atomic_store(pflag + j, ep, RLX_AGENT); wait_flags4(pflag, ep, lane); }
    vbar<0>();
    if (bid == 0 && hw == 3) {
#pragma unroll 1
      for (unsigned spins = 0; spins < (1u << 17); ++spins) {
        bool ok = true;
#pragma unroll
        for (int k = 0; k < 4; ++k) ok &= __hip_atomic_load(fl + 512 + lane * 8 + 4 + k, RLX_AGENT) == ep;
        if (__all(ok)) break;
        __builtin_amdgcn_s_sleep(2);
      }
      if (lane == 0) __hip_atomic_store(fl, ep, RLX_AGENT);
    }
  }
  if (wid < 7) {
    f32x4 in[3][4];
#pragma unroll
    for (int k = 0; k < 3; ++k) { const int src = (j + 1 + k) & 3;
#pragma unroll
      for (int e = 0; e < 4; e += 2) { const u32x4 pk = __builtin_amdgcn_raw_buffer_load_b128(prs, lane * 16, ((j * 4 + src) * 28 + wid * 2 + (e >> 1)) * 1024, 16);
        in[k][e] = (f32x4){__uint_as_float(pk[0] << 16), __uint_as_float(pk[0] & 0xffff0000u), __uint_as_float(pk[1] << 16), __uint_as_float(pk[1] & 0xffff0000u)};
        in[k][e + 1] = (f32x4){__uint_as_float(pk[2] << 16), __uint_as_float(pk[2] & 0xffff0000u), __uint_as_float(pk[3] << 16), __uint_as_float(pk[3] & 0xffff0000u)}; }
    }
    const int q = lane & 15, g = lane >> 4;
    f32x4 sum[4];
#pragma unroll
    for (int e = 0; e < 4; ++e) sum[e] = *(LAS const f32x4*)(L + QXOFF + (wid * 4 + e) * 1024 + lane * 16) + in[0][e] + (in[1][e] + in[2][e]);
#pragma unroll
    for (int pr = 0; pr < 2; ++pr)
      *(GAS u32x4*)(out + ((size_t)(b * 112 + 16 * wid + q) * 2048 + br * 1024 + c * 256 + j * 64 + pr * 32 + g * 8) * 2) = pack32(sum[2 * pr], sum[2 * pr + 1]);
  }
  STAMP(8); CSTAMP(1);
}

extern "C" void kernel_launch(void* const* d_in, const int* in_sizes, int n_in, void* d_out, int out_size, void* d_ws, size_t ws_size, hipStream_t stream) {
  (void)in_sizes; (void)n_in; (void)out_size; (void)ws_size;
  (void)hipFuncSetAttribute((const void*)fused, hipFuncAttributeMaxDynamicSharedMemorySize, LDS_BYTES);
  fused<<<256, 512, LDS_BYTES, stream>>>((const char*)d_in[0], (const char*)d_in[1], (const char*)d_in[2], (const char*)d_in[3], (const char*)d_in[4],
                                         (const char*)d_in[5], (const char*)d_in[6], (const char*)d_in[7], (const char*)d_in[8], (const char*)d_in[9], (char*)d_ws, (char*)d_out);
}
```

```cpp
#include <hip/hip_runtime.h>
#include <hip/hip_bf16.h>
#include <stdint.h>

typedef __attribute__((ext_vector_type(8))) short bf16x8;
typedef __attribute__((ext_vector_type(4))) short s16x4;
typedef __attribute__((ext_vector_type(4))) float f32x4;
typedef __attribute__((ext_vector_type(2))) float f32x2;
typedef __attribute__((ext_vector_type(16))) float f32x16;
typedef __attribute__((ext_vector_type(4))) unsigned u32x4;
typedef __attribute__((ext_vector_type(2))) __bf16 bf16x2_t;
typedef short v4i16_t __attribute__((ext_vector_type(4)));
typedef unsigned long long u64;
#define LAS __attribute__((address_space(3)))
#define GAS __attribute__((address_space(1)))
#define DI __device__ __forceinline__
#define MFMA16(a, b, c) __builtin_amdgcn_mfma_f32_16x16x32_bf16((a), (b), (c), 0, 0, 0)
#define MFMA32(a, b, c) __builtin_amdgcn_mfma_f32_32x32x16_bf16((a), (b), (c), 0, 0, 0)
#define SBAR() __builtin_amdgcn_sched_barrier(0)
#define RLX_AGENT __ATOMIC_RELAXED, __HIP_MEMORY_SCOPE_AGENT

#define W_NT 0
#define W_NT2 0
#define RB 8
#define PBF16 1
#define STAGGER 1
#define X_NT 0
#define X_NT2 0
#define KV_NT 1
#define NTN 2
#define ROT 1
#define THR 8.0f

constexpr int LDS_BYTES = 163840;
constexpr int SLOT = 16384;
constexpr int WOFF = 65536, WST = 16384;
constexpr int XOFF = 131072, XST = 7168;
constexpr int QXOFF = 65536;
constexpr int Q1OFF = 131072;
constexpr int Q0OFF = 98304;
constexpr int OS0OFF = Q0OFF, OS1OFF = 2 * SLOT;
constexpr int PFOFF = 159744;
constexpr int NSL = 6;
constexpr size_t WS_O = 0;
constexpr size_t WS_PART = 16u << 20;
constexpr size_t PART_TILE = 16 * 28 * 1024;
constexpr size_t WS_FLAG = 64u << 20;

template <int NT>
DI void glds(const void* sbase, unsigned voff, unsigned ldsdst) {
  if (NT == 1) asm volatile("s_mov_b32 m0, %2\n\ts_nop 3\n\tglobal_load_lds_dwordx4 %0, %1 nt" ::"v"(voff), "s"(sbase), "s"(ldsdst) : "memory");
  else if (NT == 2) asm volatile("s_mov_b32 m0, %2\n\ts_nop 3\n\tglobal_load_lds_dwordx4 %0, %1 sc1" ::"v"(voff), "s"(sbase), "s"(ldsdst) : "memory");
  else asm volatile("s_mov_b32 m0, %2\n\ts_nop 3\n\tglobal_load_lds_dwordx4 %0, %1" ::"v"(voff), "s"(sbase), "s"(ldsdst) : "memory");
}
template <int N> DI void vbar() { asm volatile("s_waitcnt vmcnt(%0)\n\ts_barrier" ::"i"(N) : "memory"); }
template <int N> DI void wait_vm() { asm volatile("s_waitcnt vmcnt(%0)" ::"i"(N) : "memory"); }
DI void lbar() { asm volatile("s_waitcnt lgkmcnt(0)\n\ts_barrier" ::: "memory"); }
DI void bar() { asm volatile("s_barrier" ::: "memory"); }
DI unsigned cvtpk(float lo, float hi) { const f32x2 v = {lo, hi}; return __builtin_bit_cast(unsigned, __builtin_convertvector(v, bf16x2_t)); }
#define STAMP(i) do {} while (0)
#define CSTAMP(i) do {} while (0)
#define HSTAMP(i) do {} while (0)
DI s16x4 trread(LAS const char* p) { return __builtin_bit_cast(s16x4, __builtin_amdgcn_ds_read_tr16_b64_v4i16((LAS v4i16_t*)p)); }
DI bf16x8 ldsr(LAS const char* p) { return *(LAS const bf16x8*)p; }
DI bf16x8 cat8(s16x4 a, s16x4 b) { return __builtin_shufflevector(a, b, 0, 1, 2, 3, 4, 5, 6, 7); }
DI int fsw(int row) { return (row & 3) | ((((row >> 2) ^ (row >> 3)) & 1) << 2); }
DI u64 mix64(u64 z) { z += 0x9E3779B97F4A7C15ull; z = (z ^ (z >> 30)) * 0xBF58476D1CE4E5B9ull; z = (z ^ (z >> 27)) * 0x94D049BB133111EBull; return z ^ (z >> 31); }

DI u32x4 pack32(f32x4 a, f32x4 b) {
  auto r0 = __builtin_amdgcn_permlane32_swap(cvtpk(a[0], a[1]), cvtpk(b[0], b[1]), false, false);
  auto r1 = __builtin_amdgcn_permlane32_swap(cvtpk(a[2], a[3]), cvtpk(b[2], b[3]), false, false);
  auto s0 = __builtin_amdgcn_permlane16_swap(r0[0], r0[1], false, false);
  auto s1 = __builtin_amdgcn_permlane16_swap(r1[0], r1[1], false, false);
  u32x4 o = {s0[0], s1[0], s0[1], s1[1]};
  return o;
}
DI float rowmax4(float mx) {
  auto r = __builtin_amdgcn_permlane32_swap(__float_as_uint(mx), __float_as_uint(mx), false, false);
  mx = __builtin_fmaxf(__uint_as_float(r[0]), __uint_as_float(r[1]));
  auto r2 = __builtin_amdgcn_permlane16_swap(__float_as_uint(mx), __float_as_uint(mx), false, false);
  return __builtin_fmaxf(__uint_as_float(r2[0]), __uint_as_float(r2[1]));
}
DI float max4(float m, const f32x4& c) { return __builtin_fmaxf(__builtin_fmaxf(__builtin_fmaxf(m, c[0]), c[1]), __builtin_fmaxf(c[2], c[3])); }

template <bool VM>
DI void c_stage(f32x4 (&acc)[7][4], bf16x8 (&X)[7], const bf16x8 (&W)[4], bf16x8 (&Wn)[4], LAS const char* xr, LAS const char* const (&wr)[8], int cs, int ns) {
  s16x4 lo[4];
#pragma unroll
  for (int gp = 0; gp < 28; ++gp) {
    const int mt = gp >> 2, e = gp & 3;
    acc[mt][e] = MFMA16(W[e], X[mt], acc[mt][e]);
    SBAR();
    if (gp == 0) X[6] = ldsr(xr + cs * XST + 6 * 1024);
    if (gp == 5) { if (VM) asm volatile("s_waitcnt vmcnt(0) lgkmcnt(0)\n\ts_barrier" ::: "memory"); else lbar(); }
    if (gp >= 6 && gp < 22 && (gp & 1) == 0) { const int k = (gp - 6) >> 1;
      if ((k & 1) == 0) lo[k >> 1] = trread(wr[k >> 1] + ns * WST); else Wn[k >> 1] = cat8(lo[k >> 1], trread(wr[4 + (k >> 1)] + ns * WST + 1024)); }
    if (gp >= 7 && (gp & 3) == 3) X[mt - 1] = ldsr(xr + ns * XST + (mt - 1) * 1024);
    SBAR();
  }
}
DI void c_gemm(f32x4 (&acc)[7][4], LAS char* L, int wid, int lane, bool xlin) {
  asm volatile("" : "+v"(lane));
  const int q = lane & 15, g = lane >> 4;
  LAS const char* xr = L + XOFF + (xlin ? lane * 16 : q * 64 + ((g ^ ((-(q >> 2)) & 3)) << 4));
  LAS const char* wr[8];
  { const int row = 8 * g + (q >> 2), p = q & 3, f = fsw(row), nt0 = 4 * (wid & 1);
    LAS const char* wb = L + WOFF + (wid >> 1) * 8192 + row * 256 + p * 8;
#pragma unroll
    for (int e = 0; e < 4; ++e) { wr[e] = wb + (((nt0 + e) ^ f) << 5); wr[4 + e] = wb + (((nt0 + e) ^ f ^ 4) << 5); } }
#pragma unroll
  for (int mt = 0; mt < 7; ++mt)
#pragma unroll
    for (int e = 0; e < 4; ++e) acc[mt][e] = (f32x4){0.f, 0.f, 0.f, 0.f};
  bf16x8 X[7], Wa[4], Wb[4];
  lbar();
#pragma unroll
  for (int e = 0; e < 4; ++e) Wa[e] = cat8(trread(wr[e]), trread(wr[4 + e] + 1024));
#pragma unroll
  for (int mt = 0; mt < 6; ++mt) X[mt] = ldsr(xr + mt * 1024);
#pragma unroll 1
  for (int s = 0; s < 32; s += 4) {
    c_stage<false>(acc, X, Wa, Wb, xr, wr, 0, 1);
    c_stage<false>(acc, X, Wb, Wa, xr, wr, 1, 2);
    c_stage<false>(acc, X, Wa, Wb, xr, wr, 2, 3);
    c_stage<true>(acc, X, Wb, Wa, xr, wr, 3, 0);
  }
}

constexpr int DST = 30720;
DI void c_stage128(f32x4 (&acc)[7][2], bf16x8 (&X)[14], const bf16x8 (&W)[4], bf16x8 (&Wn)[4], LAS const char* (&xr)[2], LAS const char* (&wr)[4], int dn) {
  s16x4 lo[4];
#pragma unroll
  for (int gp = 0; gp < 28; ++gp) {
    const int i = gp >> 1, e = gp & 1, kh = i / 7, mt = i % 7;
    acc[mt][e] = MFMA16(W[2 * kh + e], X[i], acc[mt][e]);
    SBAR();
    if (gp < 3) X[11 + gp] = ldsr(xr[1] + (4 + gp) * 2048);
    if (gp == 3) { xr[0] += dn; wr[0] += dn; }
    if (gp == 4) { xr[1] += dn; wr[1] += dn; }
    if (gp == 5) { wr[2] += dn; wr[3] += dn; }
    if (gp == 6) lbar();
    if (gp >= 7 && (gp & 1) == 1) { const int z = (gp - 7) >> 1; X[z] = ldsr(xr[z / 7] + (z % 7) * 2048); }
    if (gp >= 8 && gp < 24 && (gp & 1) == 0) { const int k = (gp - 8) >> 1, f = k >> 1;
      if ((k & 1) == 0) lo[f] = trread(wr[f & 1] + (f >> 1) * 8192); else Wn[f] = cat8(lo[f], trread(wr[2 + (f & 1)] + (f >> 1) * 8192 + 1024)); }
    SBAR();
  }
}
DI void c_gemm128(f32x4 (&acc)[7][2], LAS char* L, int wid, int lane) {
  asm volatile("" : "+v"(lane));
  const int q = lane & 15, g = lane >> 4;
  LAS const char* xr[2] = {L + WOFF + 16384 + q * 128 + ((g ^ (q & 7)) << 4), L + WOFF + 16384 + q * 128 + (((4 + g) ^ (q & 7)) << 4)};
  LAS const char* wr[4];
  { const int row = 8 * g + (q >> 2), p = q & 3, f = fsw(row), nt0 = 2 * wid;
    LAS const char* wb = L + WOFF + row * 256 + p * 8;
#pragma unroll
    for (int e = 0; e < 2; ++e) { wr[e] = wb + (((nt0 + e) ^ f) << 5); wr[2 + e] = wb + (((nt0 + e) ^ f ^ 4) << 5); } }
#pragma unroll
  for (int mt = 0; mt < 7; ++mt) { acc[mt][0] = (f32x4){0.f, 0.f, 0.f, 0.f}; acc[mt][1] = (f32x4){0.f, 0.f, 0.f, 0.f}; }
  bf16x8 X[14], Wa[4], Wb[4];
  lbar();
#pragma unroll
  for (int f = 0; f < 4; ++f) Wa[f] = cat8(trread(wr[f & 1] + (f >> 1) * 8192), trread(wr[2 + (f & 1)] + (f >> 1) * 8192 + 1024));
#pragma unroll
  for (int i = 0; i < 11; ++i) X[i] = ldsr(xr[i / 7] + (i % 7) * 2048);
  int sl = 0;
#pragma unroll 1
  for (int s = 0; s < 16; s += 2) {
    c_stage128(acc, X, Wa, Wb, xr, wr, sl == 2 ? -2 * DST : DST); sl = sl == 2 ? 0 : sl + 1;
    c_stage128(acc, X, Wb, Wa, xr, wr, sl == 2 ? -2 * DST : DST); sl = sl == 2 ? 0 : sl + 1;
  }
}

#define PIN(x) asm volatile("" : "+v"(x))
DI float mx3(float a, float b, float c) { return __builtin_elementwise_maximum(__builtin_elementwise_maximum(a, b), c); }
DI void vlbar() { asm volatile("s_waitcnt vmcnt(0) lgkmcnt(0)\n\ts_barrier" ::: "memory"); }

struct HA { const char* k0; const char* v0; unsigned kv[4], vv[4]; unsigned d; };
DI void h_kv(const HA& A, int T) {
  const int n = T >> 1; const unsigned so = (unsigned)(n >> 3) * (16u * 131072u) + (n & 7) * SLOT;
  const unsigned d = A.d + (T % NSL) * SLOT;
  if (T & 1) {
#pragma unroll
    for (int i = 0; i < 4; ++i) glds<KV_NT>(A.v0 + so, A.vv[i], d + i * 1024);
  } else {
#pragma unroll
    for (int i = 0; i < 4; ++i) glds<KV_NT>(A.k0 + so, A.kv[i], d + i * 1024);
  }
}
DI void h_kv_setup(HA& A, const char* k0, const char* v0, int row0  , unsigned lds0, int lane) {
  A.k0 = k0 + row0 * 256; A.v0 = v0 + row0 * 256; A.d = lds0 + row0 * 256;
#pragma unroll
  for (int i = 0; i < 4; ++i) { const int row = 4 * i + (lane >> 4); A.kv[i] = row * 256 + (((lane & 15) ^ row) << 4); A.vv[i] = row * 256 + (((lane & 15) ^ ((row & 3) << 2)) << 4); }
}
DI void h_ostore(LAS const char* src  , char* dst, int lane) {
  const __amdgpu_buffer_rsrc_t ors = __builtin_amdgcn_make_buffer_rsrc(dst, 0, XST, 0x00020000);
  u32x4 v[7];
#pragma unroll
  for (int i = 0; i < 7; ++i) v[i] = *(LAS const u32x4*)(src + i * 1024 + lane * 16);
#pragma unroll
  for (int i = 0; i < 7; ++i) __builtin_amdgcn_raw_buffer_store_b128(v[i], ors, i * 1024 + lane * 16, 0, 16);
}
DI void wait_flags4(GAS u64* f, u64 e, int lane) {
#pragma unroll 1
  for (unsigned spins = 0; spins < (1u << 17); ++spins) {
    const u64 v = __hip_atomic_load(f + (lane & 3), RLX_AGENT);
    if (__all(v == e)) break;
    __builtin_amdgcn_s_sleep(1);
  }
}
DI float xhalf_max(float m) { auto r = __builtin_amdgcn_permlane32_swap(__float_as_uint(m), __float_as_uint(m), false, false); return __builtin_fmaxf(__uint_as_float(r[0]), __uint_as_float(r[1])); }
DI float xhalf_sum(float m) { auto r = __builtin_amdgcn_permlane32_swap(__float_as_uint(m), __float_as_uint(m), false, false); return __uint_as_float(r[0]) + __uint_as_float(r[1]); }
struct AttnS { f32x16 O[4], nm, Ls; bf16x8 Qf[8], kf[8], ones; unsigned w0, w1; float mx; bool big; };
#define LGKM(n) __builtin_amdgcn_s_waitcnt(0xC07F | ((n) << 8))
#define EXP2(x) __builtin_amdgcn_exp2f(x)

template <int GK>
DI void a_grp(AttnS& A, f32x16& So, f32x16& Sn, LAS const char* (&ka)[8], LAS const char* (&va)[4], int dk) {
  constexpr int NT[18] = {0, 1, 2, 1, 2, 1, 2, 1, 2, 1, 2, 1, 0, 0, 0, 0, 0, 0};
  constexpr int NB[18] = {0, 0, 0, 0, 0, 0, 0, 0, 0, 0, 0, 1, 1, 1, 2, 1, 1, 1};
  constexpr int NM[18] = {0, 0, 0, 0, 0, 0, 0, 0, 0, 0, 0, 1, 3, 3, 1, 0, 0, 0};
  u32x4 pw[2]; s16x4 vt[16]; float e0 = 0.f, e1 = 0.f, e2 = 0.f;
  pw[0][0] = A.w0; pw[0][1] = A.w1;
  int it = 0, ib = 0, im = 0;
#pragma unroll
  for (int sl = 0; sl < 18; ++sl) {
    if (sl == 0) LGKM(4);
    if (sl == 4 && GK == 1) LGKM(4);
    if (sl == 9) LGKM(4);
    if (sl == 14) LGKM(3);
    if (sl < 8) Sn = MFMA32(A.kf[sl], A.Qf[sl], sl == 0 ? A.nm : Sn);
    else { const int u = (sl - 8) / 5, r = (sl - 8) % 5; const bf16x8 pf = __builtin_bit_cast(bf16x8, pw[u]);
      if (r == 0) A.Ls = MFMA32(A.ones, pf, A.Ls); else { const int z = 4 * u + r - 1; A.O[r - 1] = MFMA32(cat8(vt[2 * z], vt[2 * z + 1]), pf, A.O[r - 1]); } }
    SBAR();
    if (sl >= 1 && sl <= 11 && (sl & 1)) { const int p = 2 + (sl >> 1); unsigned w = cvtpk(So[2 * p], So[2 * p + 1]); PIN(w); pw[p >> 2][p & 3] = w; }
    if (sl == 0) { float x = EXP2(So[4]); PIN(x); So[4] = x; }
    if (sl <= 10) { float x = EXP2(So[5 + sl]); PIN(x); So[5 + sl] = x; }
    if (GK == 0 && sl == 0) { PIN(Sn); LGKM(0); bar(); }
#pragma unroll
    for (int k = 0; k < NT[sl]; ++k) { const int z = it >> 1;
      vt[it] = trread(va[z & 3] + GK * 8192 + (z >> 2) * 4096 + (it & 1) * 2048); ++it; }
    if (GK == 0 && sl >= 1 && sl <= 8) { ka[sl - 1] += dk; PIN(ka[sl - 1]); }
#pragma unroll
    for (int k = 0; k < NB[sl]; ++k) { A.kf[ib] = ldsr(ka[ib] + GK * 8192); ++ib; }
#pragma unroll
    for (int k = 0; k < NM[sl]; ++k) { float x = mx3(im == 0 ? Sn[0] : A.mx, Sn[2 * im], Sn[2 * im + 1]); PIN(x); A.mx = x; ++im; }
    if (sl == 14) A.big = __any(A.mx > THR);
    if (GK == 1 && sl >= 12 && sl <= 15) { va[sl - 12] += dk; PIN(va[sl - 12]); }
    if (sl == 15) { e0 = EXP2(Sn[0]); e1 = EXP2(Sn[1]); PIN(e0); PIN(e1); }
    if (sl == 16) { unsigned w = cvtpk(e0, e1); PIN(w); A.w0 = w; e2 = EXP2(Sn[2]); PIN(e2); }
    if (sl == 17) { unsigned w = cvtpk(e2, EXP2(Sn[3])); PIN(w); A.w1 = w; }
    SBAR();
  }
}
DI void a_check(AttnS& A, f32x16& Sn, bool first) {
  if (first || A.big) {
    float d = xhalf_max(A.mx);
    if (!first) d = __builtin_fmaxf(d, 0.f);
    const float f = first ? 0.f : EXP2(-d);
    A.nm -= d; Sn -= d;
#pragma unroll
    for (int dt = 0; dt < 4; ++dt) A.O[dt] *= f;
    A.Ls *= f;
    A.w0 = cvtpk(EXP2(Sn[0]), EXP2(Sn[1])); A.w1 = cvtpk(EXP2(Sn[2]), EXP2(Sn[3]));
  }
}
DI void a_loadq(AttnS& A, LAS const char* Qi, int wid, int lane) {
  const int q = lane & 31, h = lane >> 5; int mt = 2 * wid + (q >> 4); mt = mt > 6 ? 6 : mt;
  LAS const char* p = Qi + mt * 4096 + (h * 16 + (q & 15)) * 16;
#pragma unroll
  for (int s = 0; s < 8; ++s) A.Qf[s] = ldsr(p + (s >> 1) * 1024 + (s & 1) * 512);
#pragma unroll
  for (int i = 0; i < 16; ++i) A.nm[i] = 0.f;
}
DI void c_attn(LAS char* L, int wid, int lane, int nper  ) {
  asm volatile("" : "+v"(lane));
  const int r = lane & 31, h = lane >> 5;
  LAS const char* ka[8]; LAS const char* va[4];
#pragma unroll
  for (int s = 0; s < 8; ++s) ka[s] = L + r * 256 + (((h ^ (r & 15)) << 4) ^ (s << 5));
  { const int db = (lane >> 4) & 1, qq = (lane & 15) >> 2, p = lane & 3;
#pragma unroll
    for (int dt = 0; dt < 4; ++dt) va[dt] = L + SLOT + (4 * h + qq) * 256 + (((2 * dt + db) ^ (qq << 1)) << 5) + p * 8; }
  AttnS A; f32x16 SA, SB;
#pragma unroll
  for (int dt = 0; dt < 4; ++dt)
#pragma unroll
    for (int i = 0; i < 16; ++i) A.O[dt][i] = 0.f;
#pragma unroll
  for (int i = 0; i < 16; ++i) A.Ls[i] = 0.f;
  A.ones = (bf16x8){0x3F80, 0x3F80, 0x3F80, 0x3F80, 0x3F80, 0x3F80, 0x3F80, 0x3F80}; PIN(A.ones);
  a_loadq(A, L + Q0OFF, wid, lane);
  bar();
#pragma unroll
  for (int s = 0; s < 8; ++s) SA = MFMA32(ldsr(ka[s]), A.Qf[s], s == 0 ? A.nm : SA);
#pragma unroll
  for (int s = 0; s < 8; ++s) A.kf[s] = ldsr(ka[s] + 8192);
  A.mx = SA[0];
#pragma unroll
  for (int i = 0; i < 16; i += 2) A.mx = mx3(A.mx, SA[i], SA[i + 1]);
  a_check(A, SA, true);
  int ks = 0;
#pragma unroll 1
  for (int n = 0; n < nper; ++n) {
    const int dk = ks == 2 ? -4 * SLOT : 2 * SLOT;
    a_grp<0>(A, SA, SB, ka, va, dk);
    a_check(A, SB, false);
    if (n == 7) a_loadq(A, L + Q1OFF, wid, lane);
    a_grp<1>(A, SB, SA, ka, va, dk);
    if ((n & 7) == 7) {
      const float inv = __builtin_amdgcn_rcpf(A.Ls[0]);
      LAS char* os = L + (nper == 8 ? 0 : (n == 7 ? OS0OFF : OS1OFF)) + 2 * wid * 1024 + ((r >> 4) * 64 + h * 16 + (r & 15)) * 16;
      if (wid < 3 || r < 16) {
#pragma unroll
        for (int dt = 0; dt < 4; ++dt)
#pragma unroll
          for (int g = 0; g < 4; g += 2) {
            auto x0 = __builtin_amdgcn_permlane32_swap(cvtpk(A.O[dt][4 * g] * inv, A.O[dt][4 * g + 1] * inv), cvtpk(A.O[dt][4 * g + 4] * inv, A.O[dt][4 * g + 5] * inv), false, false);
            auto x1 = __builtin_amdgcn_permlane32_swap(cvtpk(A.O[dt][4 * g + 2] * inv, A.O[dt][4 * g + 3] * inv), cvtpk(A.O[dt][4 * g + 6] * inv, A.O[dt][4 * g + 7] * inv), false, false);
            const u32x4 o = {x0[0], x1[0], x0[1], x1[1]};
            *(LAS u32x4*)(os + dt * XST + g * 256) = o;
          }
      }
    }
    if (n < nper - 1) a_check(A, SA, n == 7);
    ks = ks == 2 ? 0 : ks + 1;
  }
}

struct HG { const char* w; const char* x; unsigned ldw, xstep; unsigned wv[4], xv[2]; unsigned wd, xd, xd1; };
DI void h_setup(HG& H, const char* x, unsigned ldx, const char* w0, const char* w1, unsigned ldw, unsigned lds0, int hw, int lane) {
  H.w = ((hw >> 1) ? w1 : w0) + (size_t)(16 * (hw & 1)) * ldw; H.ldw = ldw;
#pragma unroll
  for (int i = 0; i < 4; ++i) { const int row = 4 * i + (lane >> 4); H.wv[i] = row * ldw + (((lane & 15) ^ (fsw(row) << 1)) << 4); }
  if (ldx) {
    H.x = x + (size_t)(32 * hw) * ldx; H.xstep = 64;
    const unsigned xo = (lane >> 2) * ldx + (((lane & 3) ^ ((-(lane >> 4)) & 3)) << 4);
    H.xv[0] = xo; H.xv[1] = xo + (hw < 3 ? 16 * ldx : 0);
  } else {
    H.x = x + hw * 2048; H.xstep = XST; H.xv[0] = lane * 16; H.xv[1] = lane * 16 + (hw < 3 ? 1024 : 0);
  }
  H.wd = lds0 + WOFF + hw * 4096; H.xd = lds0 + XOFF + hw * 2048; H.xd1 = hw < 3 ? 1024 : 0;
}
template <int NT, int XP>
DI void h_issue(const HG& H, int kb  , int slot) {
  const char* w = H.w + (size_t)kb * 32 * H.ldw; const char* x = H.x + kb * H.xstep;
#pragma unroll
  for (int i = 0; i < 4; ++i) glds<NT>(w, H.wv[i], H.wd + slot * WST + i * 1024);
  glds<XP>(x, H.xv[0], H.xd + slot * XST); glds<XP>(x, H.xv[1], H.xd + slot * XST + H.xd1);
}
struct HG2 { const char* w; const char* x; unsigned wv[4], xv[4]; unsigned wd, xd, xd3; };
DI void h_setup128(HG2& H, const char* x, const char* w, unsigned lds0, int hw, int lane) {
  H.w = w + (size_t)(16 * hw) * 8192;
#pragma unroll
  for (int i = 0; i < 4; ++i) { const int row = 4 * i + (lane >> 4); H.wv[i] = row * 8192 + (((lane & 15) ^ (fsw(row) << 1)) << 4); }
  const int p0 = hw < 2 ? 4 * hw : 8 + 3 * (hw - 2);
  H.x = x + (size_t)(8 * p0) * 2048;
  H.xv[0] = (lane >> 3) * 2048 + (((lane & 7) ^ ((lane >> 3) & 7)) << 4);
  H.wd = lds0 + WOFF + hw * 4096; H.xd = lds0 + WOFF + 16384 + p0 * 1024; H.xd3 = hw < 2 ? 0u : lds0 + PFOFF + hw * 256;
}
template <int NT, int XP>
DI void h_issue128(const HG2& H, int kd  , int slot) {
  const char* w = H.w + (size_t)kd * 64 * 8192; const char* x = H.x + kd * 128;
#pragma unroll
  for (int i = 0; i < 4; ++i) glds<NT>(w, H.wv[i], H.wd + slot * DST + i * 1024);
#pragma unroll
  for (int i = 0; i < 3; ++i) glds<XP>(x + i * 16384, H.xv[0], H.xd + slot * DST + i * 1024);
  if (H.xd3 == 0) glds<XP>(x + 3 * 16384, H.xv[0], H.xd + slot * DST + 3072);
  else asm volatile("s_mov_b32 m0, %2\n\ts_nop 3\n\tglobal_load_lds_dword %0, %1" ::"v"(0u), "s"(x), "s"(H.xd3) : "memory");
}

__global__ __launch_bounds__(512, 2) void fused(const char* xA, const char* kA, const char* vA, const char* wqA, const char* woA,
                                                const char* xB, const char* kB, const char* vB, const char* wqB, const char* woB, char* ws, char* out) {
  extern __shared__ __attribute__((aligned(16))) char lds[];
  LAS char* L = (LAS char*)lds; const unsigned lds0 = (unsigned)(uintptr_t)L;
  const int tid = threadIdx.x, lane = tid & 63; const int wid = __builtin_amdgcn_readfirstlane(tid >> 6);
  const int bid = blockIdx.x, xcd = bid & 7, idx = bid >> 3, br = xcd >> 2, j = xcd & 3, b = idx >> 2, c = idx & 3, h0 = 4 * j + c, h1 = h0 + 16;
  char* Ob = ws + WS_O + (size_t)(br * 8 + b) * (128 * XST);
  const int tile = (br * 8 + b) * 4 + c;
  const __amdgpu_buffer_rsrc_t prs = __builtin_amdgcn_make_buffer_rsrc(ws + WS_PART + (size_t)tile * PART_TILE, 0, (int)PART_TILE, 0x00020000);
  STAMP(0); CSTAMP(0);
  const int hw = wid & 3;
  const char* kc = (br ? kB : kA) + (size_t)(b * 32 + h0) * 131072; const char* vc = (br ? vB : vA) + (size_t)(b * 32 + h0) * 131072;
  GAS u64* fl = (GAS u64*)(ws + WS_FLAG);
  GAS u64* oflag = fl + 512 + ((br * 8 + b) * 4 + j) * 8; GAS u64* pflag = fl + 2048 + tile * 8;
  const int r8 = RB == 16 ? (2 * b + 4 * c) & 15 : RB == 8 ? (b + 2 * c) & 7 : (b + c) & (RB - 1);
#define KROT(s) (((s) & ~(RB - 1)) | (((s) + r8) & (RB - 1)))
  u64 ep = 0;
  const char* wo = (br ? woB : woA) + (size_t)(512 * j) * 2048 + c * 512;
  const bool grpB = STAGGER && (b & 1);
  const float cs = 0.08838834764831845f * 1.4426950408889634f;
  if (wid < 4) {
#pragma unroll 1
    for (int ph = 0; ph < (grpB ? 2 : 1); ++ph) {
      int lz = lane; asm volatile("" : "+v"(lz));
      if (grpB) {
        f32x4 acc[7][2];
        c_gemm128(acc, L, wid, lane);
        lbar();
#pragma unroll
        for (int mt = 0; mt < 7; ++mt) *(LAS u32x4*)(L + Q0OFF + (mt * 4 + wid) * 1024 + lz * 16) = pack32(acc[mt][0] * cs, acc[mt][1] * cs);
      } else {
        f32x4 acc[7][4];
        c_gemm(acc, L, wid, lane, false);
        lbar();
        LAS char* qi = L + ((wid >> 1) ? Q1OFF : Q0OFF) + 2 * (wid & 1) * 1024 + lz * 16;
#pragma unroll
        for (int mt = 0; mt < 7; ++mt)
#pragma unroll
          for (int e = 0; e < 2; ++e) *(LAS u32x4*)(qi + (mt * 4 + e) * 1024) = pack32(acc[mt][2 * e] * cs, acc[mt][2 * e + 1] * cs);
      }
      lbar();
      STAMP(1 + ph);
      c_attn(L, wid, lane, grpB ? 8 : 16);
      if (ph == 0) STAMP(3);
    }
    STAMP(4);
  } else {
    const char* x = (br ? xB : xA) + (size_t)b * 112 * 2048; const char* wq = br ? wqB : wqA;
    HA A; h_kv_setup(A, kc, vc, 16 * hw, lds0, lane);
    if (!grpB) {
      HG H; h_setup(H, x, 2048, wq + h0 * 256, wq + h1 * 256, 8192, lds0, hw, lane);
      h_issue<W_NT, X_NT>(H, KROT(0), 0); h_issue<W_NT, X_NT>(H, KROT(1), 1);
      vbar<6>(); h_issue<W_NT2, X_NT2>(H, KROT(2), 2);
      vbar<6>(); h_issue<W_NT2, X_NT2>(H, KROT(3), 3); h_issue<W_NT2, X_NT2>(H, KROT(4), 0);
#pragma unroll 1
      for (int s = 1; s < 28; ++s) { vbar<12>();
        if (((s + 4) & (RB - 1)) < NTN) h_issue<W_NT, X_NT>(H, KROT(s + 4), s & 3); else h_issue<W_NT2, X_NT2>(H, KROT(s + 4), s & 3); }
      vbar<12>(); h_kv(A, 0);
      vbar<10>(); h_kv(A, 1);
      vbar<8>();
      bar(); h_kv(A, 2);
      bar();
      { u64 seed; asm volatile("s_load_dwordx2 %0, %1, 0x0\n\ts_waitcnt lgkmcnt(0)" : "=s"(seed) : "s"(fl) : "memory"); ep = mix64(seed); }
      bar();
      vbar<8>(); h_kv(A, 3); h_kv(A, 4);
#pragma unroll 1
      for (int n = 0; n < 13; ++n) {
        vbar<8>();
        if (n == 8) h_ostore(L + OS0OFF + hw * XST, Ob + (h0 * 4 + hw) * XST, lane);
        h_kv(A, 2 * n + 5); h_kv(A, 2 * n + 6);
        if (n == 9 && tid == 256) __hip_atomic_store(oflag + c, ep, RLX_AGENT);
      }
      vbar<8>(); h_kv(A, 31);
      vbar<4>();
      wait_flags4(oflag, ep, lane);
      bar();
    } else {
      const int r4 = ((b >> 1) + c) & 3;
#define KROT4(s) (((s) & ~3) | (((s) + r4) & 3))
      HG2 H; h_setup128(H, x, wq + h0 * 256, lds0, hw, lane);
      h_issue128<W_NT, X_NT>(H, KROT4(0), 0); h_issue128<W_NT2, X_NT2>(H, KROT4(1), 1);
      { u64 seed; asm volatile("s_load_dwordx2 %0, %1, 0x0\n\ts_waitcnt lgkmcnt(0)" : "=s"(seed) : "s"(fl) : "memory"); ep = mix64(seed); }
#pragma unroll 1
      for (int ph = 0; ph < 2; ++ph) {
        vbar<8>();
        h_issue128<W_NT2, X_NT2>(H, KROT4(2), 2);
        if (ph) h_ostore(L + hw * XST, Ob + (h0 * 4 + hw) * XST, lane);
        int sl = 0;
#pragma unroll 1
        for (int s = 0; s < 13; ++s) {
          vbar<8>();
          if (((s + 3) & 3) == 0) h_issue128<W_NT, X_NT>(H, KROT4(s + 3), sl); else h_issue128<W_NT2, X_NT2>(H, KROT4(s + 3), sl);
          sl = sl == 2 ? 0 : sl + 1;
          if (ph && s == 2 && tid == 256) __hip_atomic_store(oflag + c, ep, RLX_AGENT);
        }
        vbar<8>(); h_kv(A, 0);
        vbar<4>(); h_kv(A, 1);
        bar();
        bar(); h_kv(A, 2);
        bar();
        vbar<8>(); h_kv(A, 3); h_kv(A, 4);
#pragma unroll 1
        for (int n = 0; n < 5; ++n) { vbar<8>();
          h_kv(A, 2 * n + 5); h_kv(A, 2 * n + 6); }
        vbar<8>(); h_kv(A, 15);
        vbar<4>();
        if (ph == 0) {
          H.w += 16 * 256;
          vbar<0>();
          h_issue128<W_NT, X_NT>(H, KROT4(0), 0); h_issue128<W_NT2, X_NT2>(H, KROT4(1), 1);
          A.k0 += 16 * 131072; A.v0 += 16 * 131072;
        } else {
          wait_flags4(oflag, ep, lane);
          bar();
        }
      }
    }
  }
  if (wid < 4) {
    f32x4 acc[7][4];
    c_gemm(acc, L, wid, lane, true);
    STAMP(5);
    lbar();
    if (wid != j) {
#pragma unroll
      for (int mt = 0; mt < 7; ++mt)
#pragma unroll
        for (int e = 0; e < 4; e += 2) { const u32x4 pk = {cvtpk(acc[mt][e][0], acc[mt][e][1]), cvtpk(acc[mt][e][2], acc[mt][e][3]), cvtpk(acc[mt][e + 1][0], acc[mt][e + 1][1]), cvtpk(acc[mt][e + 1][2], acc[mt][e + 1][3])};
          __builtin_amdgcn_raw_buffer_store_b128(pk, prs, lane * 16 + ((wid * 4 + j) * 28 + mt * 2 + (e >> 1)) * 1024, 0, 16); }
    } else {
#pragma unroll
      for (int mt = 0; mt < 7; ++mt)
#pragma unroll
        for (int e = 0; e < 4; ++e) *(LAS f32x4*)(L + QXOFF + (mt * 4 + e) * 1024 + lane * 16) = acc[mt][e];
    }
    vlbar();
    STAMP(6);
    bar();
    STAMP(7);
  } else {
    int hz = hw, lz = lane; asm volatile("" : "+s"(hz)); asm volatile("" : "+v"(lz));
    HG H; h_setup(H, Ob + j * 16 * XST, 0, wo, wo + 256, 2048, lds0, hz, lz);
    u64 fv = 0;
    h_issue<W_NT, 2>(H, KROT(0), 0); h_issue<W_NT, 2>(H, KROT(1), 1);
    vbar<6>();
    h_issue<W_NT2, 2>(H, KROT(2), 2);
    h_ostore(L + (grpB ? 0 : OS1OFF) + hz * XST, Ob + (h1 * 4 + hz) * XST, lz);
    vbar<13>(); h_issue<W_NT2, 2>(H, KROT(3), 3); h_issue<W_NT2, 2>(H, KROT(4), 0);
#pragma unroll 1
    for (int s = 1; s < 28; ++s) {
      vbar<12>();
      if (s == 9) asm volatile("global_load_dwordx2 %0, %1, %2 sc1" : "=v"(fv) : "v"((lz & 3) * 8 + 32), "s"(oflag) : "memory");
      if (s == 12) { asm volatile("" : "+v"(fv)); if (!__all(fv == ep)) wait_flags4(oflag + 4, ep, lz); }
      const int n = s + 4, kb = (n >> 4) * 48 + KROT(n);
      if ((n & (RB - 1)) < NTN) h_issue<W_NT, 2>(H, kb, s & 3); else h_issue<W_NT2, 2>(H, kb, s & 3);
      if (s == 1 && tid == 256) __hip_atomic_store(oflag + 4 + c, ep, RLX_AGENT);
    }
    vbar<12>(); vbar<6>(); vbar<0>(); bar();
    bar();
    bar();
    if (hw == 0) { if (lane == 0) __hip_atomic_store(pflag + j, ep, RLX_AGENT); wait_flags4(pflag, ep, lane); }
    vbar<0>();
    if (bid == 0 && hw == 3) {
#pragma unroll 1
      for (unsigned spins = 0; spins < (1u << 17); ++spins) {
        bool ok = true;
#pragma unroll
        for (int k = 0; k < 4; ++k) ok &= __hip_atomic_load(fl + 512 + lane * 8 + 4 + k, RLX_AGENT) == ep;
        if (__all(ok)) break;
        __builtin_amdgcn_s_sleep(2);
      }
      if (lane == 0) __hip_atomic_store(fl, ep, RLX_AGENT);
    }
  }
  if (wid < 7) {
    f32x4 in[3][4];
#pragma unroll
    for (int k = 0; k < 3; ++k) { const int src = (j + 1 + k) & 3;
#pragma unroll
      for (int e = 0; e < 4; e += 2) { const u32x4 pk = __builtin_amdgcn_raw_buffer_load_b128(prs, lane * 16, ((j * 4 + src) * 28 + wid * 2 + (e >> 1)) * 1024, 16);
        in[k][e] = (f32x4){__uint_as_float(pk[0] << 16), __uint_as_float(pk[0] & 0xffff0000u), __uint_as_float(pk[1] << 16), __uint_as_float(pk[1] & 0xffff0000u)};
        in[k][e + 1] = (f32x4){__uint_as_float(pk[2] << 16), __uint_as_float(pk[2] & 0xffff0000u), __uint_as_float(pk[3] << 16), __uint_as_float(pk[3] & 0xffff0000u)}; }
    }
    const int q = lane & 15, g = lane >> 4;
    f32x4 sum[4];
#pragma unroll
    for (int e = 0; e < 4; ++e) sum[e] = *(LAS const f32x4*)(L + QXOFF + (wid * 4 + e) * 1024 + lane * 16) + in[0][e] + (in[1][e] + in[2][e]);
#pragma unroll
    for (int pr = 0; pr < 2; ++pr)
      *(GAS u32x4*)(out + ((size_t)(b * 112 + 16 * wid + q) * 2048 + br * 1024 + c * 256 + j * 64 + pr * 32 + g * 8) * 2) = pack32(sum[2 * pr], sum[2 * pr + 1]);
  }
  STAMP(8); CSTAMP(1);
}

extern "C" void kernel_launch(void* const* d_in, const int* in_sizes, int n_in, void* d_out, int out_size, void* d_ws, size_t ws_size, hipStream_t stream) {
  (void)in_sizes; (void)n_in; (void)out_size; (void)ws_size;
  (void)hipFuncSetAttribute((const void*)fused, hipFuncAttributeMaxDynamicSharedMemorySize, LDS_BYTES);
  fused<<<256, 512, LDS_BYTES, stream>>>((const char*)d_in[0], (const char*)d_in[1], (const char*)d_in[2], (const char*)d_in[3], (const char*)d_in[4],
                                         (const char*)d_in[5], (const char*)d_in[6], (const char*)d_in[7], (const char*)d_in[8], (const char*)d_in[9], (char*)d_ws, (char*)d_out);
}
```
